```python
import torch

def _softmax(x, axis=-1):
    return torch.softmax(x, dim=axis)
def _gelu(x):
    return torch.nn.functional.gelu(x, approximate="tanh")
def _silu(x):
    return torch.nn.functional.silu(x)
def _sigmoid(x):
    return torch.sigmoid(x)
def _layernorm(x, axis=-1, eps=1e-5):
    m = x.mean(dim=axis, keepdim=True)
    v = x.var(dim=axis, keepdim=True, unbiased=False)
    return (x - m) / torch.sqrt(v + eps)
def _scatter_add(tgt, idx, src, axis=0):
    return tgt.clone().index_add_(axis, idx, src)
def _segment_sum(src, seg, n):
    out = torch.zeros((n,) + src.shape[1:], dtype=src.dtype)
    return out.index_add_(0, seg, src)
def _pad(x, pw):
    flat = tuple(v for pair in reversed(pw) for v in pair)
    return torch.nn.functional.pad(x, flat)

kernel_name = 'recurrent_scan__mha_block__glu_gated_geglu__prompt'

def setup_inputs():

    _w = (lambda a: (lambda t: t.to(torch.bfloat16) if t.is_floating_point() else t)(torch.as_tensor(a)))
    def _A_setup():
        B, S, D, H = 1, 2048, 512, 32
        return {
            "x": _w((lambda *s: torch.randn(*s, dtype=torch.bfloat16) * 0.05)(B, S, D)),
            "wqkv": _w((lambda *s: torch.randn(*s, dtype=torch.bfloat16) * 0.2)(D, 3 * D)),
            "wo": _w((lambda *s: torch.randn(*s, dtype=torch.bfloat16) * 0.2)(D, D)),
        }
    _A_in = _A_setup()

    def _B_setup():
        B, S, Dm, Df = 1, 2048, 512, 1792
        return {
            "x": _w((lambda *s: torch.randn(*s, dtype=torch.bfloat16) * 0.05)(B, S, Dm)),
            "w_gate": _w((lambda *s: torch.randn(*s, dtype=torch.bfloat16) * 0.1)(Dm, Df) * (Dm ** -0.5)),
            "w_up": _w((lambda *s: torch.randn(*s, dtype=torch.bfloat16) * 0.1)(Dm, Df) * (Dm ** -0.5)),
            "w_down": _w((lambda *s: torch.randn(*s, dtype=torch.bfloat16) * 0.1)(Df, Dm) * (Df ** -0.5)),
        }
    _B_in = _B_setup()

    return {
        "A_x": _A_in["x"],
        "A_wqkv": _A_in["wqkv"],
        "A_wo": _A_in["wo"],
        "B_x": _B_in["x"],
        "B_w_gate": _B_in["w_gate"],
        "B_w_up": _B_in["w_up"],
        "B_w_down": _B_in["w_down"],
    }

def reference(A_x, A_wqkv, A_wo, B_x, B_w_gate, B_w_up, B_w_down):
    def _A_ref(x, wqkv, wo):
        B, S, D = x.shape
        H = 32
        Dh = D // H
        qkv = (lambda x, *d: x.permute(*d))((x @ wqkv).reshape(B, S, 3, H, Dh), 2, 0, 3, 1, 4)
        q, k, v = qkv[0], qkv[1], qkv[2]
        scores = torch.einsum("bhqd,bhkd->bhqk", q, k) * (Dh ** -0.5)
        p = _softmax(scores, axis=-1)
        o = torch.einsum("bhqk,bhkd->bhqd", p, v)
        return (lambda x, *d: x.permute(*d))(o, 0, 2, 1, 3).reshape(B, S, D) @ wo

    def _B_ref(x, w_gate, w_up, w_down):
        g = torch.einsum("bsd,df->bsf", x, w_gate)
        u = torch.einsum("bsd,df->bsf", x, w_up)
        h = _gelu(g) * u
        return torch.einsum("bsf,fd->bsd", h, w_down)

    x = A_x
    xn = _layernorm(x, axis=-1)
    s = _A_ref(xn, A_wqkv, A_wo)
    g = _silu(_B_ref(xn, B_w_gate, B_w_up, B_w_down))
    return x + s * g
```

```cpp
#include <hip/hip_runtime.h>
#include <stdint.h>

typedef __attribute__((ext_vector_type(8))) __bf16 bf16x8;
typedef __attribute__((ext_vector_type(2))) __bf16 bf16x2;
typedef __attribute__((ext_vector_type(16))) float f32x16;
typedef __attribute__((ext_vector_type(4))) float f32x4;
typedef __attribute__((ext_vector_type(2))) float f32x2;
typedef __attribute__((ext_vector_type(4))) unsigned u32x4;
typedef __attribute__((ext_vector_type(2))) unsigned u32x2;
typedef __attribute__((ext_vector_type(4))) short v4i16_t;
typedef __attribute__((address_space(3))) char* lds_ptr;
typedef __attribute__((address_space(3))) const u32x4* lds_u32x4_ptr;
typedef __attribute__((address_space(3))) const f32x4* lds_f32x4_ptr;
typedef __attribute__((address_space(3))) float* lds_f32_ptr;
typedef __attribute__((address_space(3))) v4i16_t* lds_tr_ptr;

#define DEV __device__ __forceinline__

constexpr int SEQ = 2048, DM = 512, DF = 1792;
constexpr int NTB = SEQ / 32;
constexpr int NKG = DF / 16;
constexpr int VT = 1088;
constexpr size_t WS_Q = 0;
constexpr size_t WS_K = WS_Q + 32u * NTB * 1024;
constexpr size_t WS_V = WS_K + 32u * NTB * 1024;
constexpr size_t WS_H = WS_V + 32u * NTB * VT + 4096;
constexpr size_t WS_O = WS_H + (size_t)NTB * NKG * 1024;

DEV f32x16 mfma(bf16x8 a, bf16x8 b, f32x16 c) { return __builtin_amdgcn_mfma_f32_32x32x16_bf16(a, b, c, 0, 0, 0); }
DEV unsigned pack2(float lo, float hi) {
  bf16x2 r = {(__bf16)lo, (__bf16)hi};
  return __builtin_bit_cast(unsigned, r);
}
DEV bf16x8 as_frag(u32x4 v) { return __builtin_bit_cast(bf16x8, v); }
DEV bf16x8 pack8(float a0, float a1, float a2, float a3, float a4, float a5, float a6, float a7) {
  u32x4 r = {pack2(a0, a1), pack2(a2, a3), pack2(a4, a5), pack2(a6, a7)};
  return as_frag(r);
}
DEV u32x4 lds128(lds_ptr p) { return *(lds_u32x4_ptr)p; }
DEV f32x4 ldsf4(lds_ptr p) { return *(lds_f32x4_ptr)p; }
DEV bf16x8 lds_tr8(lds_ptr p0, lds_ptr p1) {
  v4i16_t a = __builtin_amdgcn_ds_read_tr16_b64_v4i16((lds_tr_ptr)p0);
  v4i16_t b = __builtin_amdgcn_ds_read_tr16_b64_v4i16((lds_tr_ptr)p1);
  u32x2 x = __builtin_bit_cast(u32x2, a), y = __builtin_bit_cast(u32x2, b);
  u32x4 r = {x[0], x[1], y[0], y[1]};
  return as_frag(r);
}
DEV void glds16(uint32_t voff, const void* sbase, uint32_t ldsaddr) {
  asm volatile("s_mov_b32 m0, %2\n\ts_nop 0\n\tglobal_load_lds_dwordx4 %0, %1" ::"v"(voff), "s"(sbase), "s"(ldsaddr) : "memory");
}
DEV void glds16_q(uint32_t voff, const void* sbase, uint32_t ldsaddr) {
  asm volatile("s_mov_b32 m0, %2\n\ts_mov_b64 exec, 15\n\tglobal_load_lds_dwordx4 %0, %1\n\ts_mov_b64 exec, -1" ::"v"(voff), "s"(sbase), "s"(ldsaddr) : "memory");
}
template <int N> DEV void wait_vm() { asm volatile("s_waitcnt vmcnt(%0)" ::"n"(N) : "memory"); }
DEV void bar() { asm volatile("s_waitcnt lgkmcnt(0)\n\ts_barrier" ::: "memory"); }
DEV void wait_lgkm0() { asm volatile("s_waitcnt lgkmcnt(0)" ::: "memory"); }
DEV float swap_add(float v) {
  auto r = __builtin_amdgcn_permlane32_swap(__float_as_uint(v), __float_as_uint(v), false, false);
  return __uint_as_float(r[0]) + __uint_as_float(r[1]);
}
DEV float swap_max(float v) {
  auto r = __builtin_amdgcn_permlane32_swap(__float_as_uint(v), __float_as_uint(v), false, false);
  return fmaxf(__uint_as_float(r[0]), __uint_as_float(r[1]));
}
DEV float dot2(unsigned a, unsigned b, float c) {
  return __builtin_amdgcn_fdot2_f32_bf16(__builtin_bit_cast(bf16x2, a), __builtin_bit_cast(bf16x2, b), c, false);
}
DEV uint32_t lds_addr(lds_ptr p) { return (uint32_t)(uintptr_t)p; }
DEV int uniform(int v) { return __builtin_amdgcn_readfirstlane(v); }

constexpr int G1_X = 0, G1_G = 8192, G1_U = G1_G + 7168 + 128, G1_W = G1_U + 7168, G1_STAGE = G1_W + 6144;
constexpr int G1_NS = 5, G1_NSTAGE = 16;
constexpr int G1_CSP = G1_NS * G1_STAGE;
constexpr int G1_STAT = G1_CSP + 4 * 320 * 4;
constexpr int G1_RS = G1_STAT + 2 * 128 * 4;
constexpr int G1_RM = G1_RS + 512;
constexpr int G1_CS = G1_RM + 512;
constexpr int G1_LDS = G1_CS + 320 * 4;

struct G1Dma {
  const char* base[4];
  uint32_t step[4];
  uint32_t voff[4];
  uint32_t ldst[4];
};

DEV float gelu_mul(float g, float u) {
  const float c1 = -2.0f * 0.7978845608028654f * 1.4426950408889634f;
  const float c2 = c1 * 0.044715f;
  float z = g * __builtin_fmaf(g * g, c2, c1);
  float e = __builtin_amdgcn_exp2f(z);
  return g * u * __builtin_amdgcn_rcpf(1.0f + e);
}

template <int CH>
DEV void g1_body(lds_ptr smem, const char* x, const char* wqkv, const char* wg, const char* wu, char* ws, int pi, int cj, int w,
                 int lane) {
  const int tb = w & 3, hi = lane >> 5, l31 = lane & 31;
  G1Dma d;
  {
    const int t = 16 * w + (lane >> 2);
    d.base[0] = x + (size_t)pi * 128 * 1024;
    d.step[0] = 64;
    d.voff[0] = t * 1024 + (((lane & 3) ^ ((t >> 2) & 3)) << 4);
    d.ldst[0] = G1_X + w * 1024;
#pragma unroll
    for (int q = 1; q < 4; ++q) {
      const int idx = w + 8 * q;
      if (idx < 22) {
        const bool isu = idx >= 15;
        const int i = isu ? idx - 15 : idx - 8;
        const int o = i * 1024 + lane * 16, row = o / 224, ch = (o - row * 224);
        d.base[q] = (isu ? wu : wg) + cj * 224;
        d.step[q] = 32 * 3584;
        d.voff[q] = row * 3584 + ch;
        d.ldst[q] = (isu ? G1_U : G1_G) + i * 1024;
      } else {
        const int i = (idx < 28 ? idx : 27) - 22;
        const int o = i * 1024 + lane * 16, row = o / 192, c = (o - row * 192);
        d.base[q] = wqkv + cj * 64;
        d.step[q] = 32 * 3072;
        d.voff[q] = row * 3072 + (c >> 6) * 1024 + (c & 63);
        d.ldst[q] = G1_W + i * 1024;
      }
    }
  }
  const uint32_t lbase = lds_addr(smem);
  auto issue = [&](int s, int slot) {
    const uint32_t so = lbase + slot * G1_STAGE;
#pragma unroll
    for (int q = 0; q < 4; ++q) {
      if (q < 3 || CH == 0) glds16(d.voff[q], d.base[q] + (size_t)d.step[q] * s, so + d.ldst[q]);
    }
  };
  constexpr int PER = CH == 0 ? 4 : 3;
#pragma unroll
  for (int s = 0; s < G1_NS - 1; ++s) issue(s, s);

  const int g16 = (lane >> 4) & 1, q4 = (lane >> 2) & 3, p4 = lane & 3;
  int xo[2], wo_[2], go[2];
#pragma unroll
  for (int kk = 0; kk < 2; ++kk) {
    const int ks = kk ^ (tb & 1);
    const int t = 32 * tb + l31;
    xo[kk] = G1_X + t * 64 + (((2 * ks + hi) ^ ((t >> 2) & 3)) << 4);
    const int row = 16 * ks + 8 * hi + q4;
    wo_[kk] = G1_W + row * 192 + 32 * g16 + 8 * p4;
    go[kk] = (p4 < 2 ? G1_G : G1_U) + row * 224 + 16 * g16 + 8 * (p4 & 1);
  }

  f32x16 acc[5];
#pragma unroll
  for (int b = 0; b < 5; ++b)
#pragma unroll
    for (int r = 0; r < 16; ++r) acc[b][r] = 0.f;
  float st = 0.f, cs[5] = {0.f, 0.f, 0.f, 0.f, 0.f};
  const unsigned ones = 0x3F803F80u;

  int slot = 0;
  for (int s = 0; s < G1_NSTAGE; ++s) {
    if (s < G1_NSTAGE - 3) wait_vm<3 * PER>();
    else if (s == G1_NSTAGE - 3) wait_vm<2 * PER>();
    else if (s == G1_NSTAGE - 2) wait_vm<PER>();
    else wait_vm<0>();
    bar();
    if (s + G1_NS - 1 < G1_NSTAGE) issue(s + G1_NS - 1, slot == 0 ? G1_NS - 1 : slot - 1);
    lds_ptr sb = smem + slot * G1_STAGE;
    const bool do_cs = (s & 1) == (tb >> 1);
#pragma unroll
    for (int kk = 0; kk < 2; ++kk) {
      const u32x4 xr = lds128(sb + xo[kk]);
      bf16x8 wf[5];
#pragma unroll
      for (int b = 0; b < 5; ++b) {
        if (CH == 0 && b < 3) wf[b] = lds_tr8(sb + wo_[kk] + b * 64, sb + wo_[kk] + b * 64 + 4 * 192);
        else {
          const int gb = CH == 0 ? b - 3 : b + 2;
          wf[b] = lds_tr8(sb + go[kk] + gb * 32, sb + go[kk] + gb * 32 + 4 * 224);
        }
      }
      const bf16x8 xf = as_frag(xr);
#pragma unroll
      for (int b = 0; b < 5; ++b) acc[b] = (CH == 0 && b == 2) ? mfma(xf, wf[b], acc[b]) : mfma(wf[b], xf, acc[b]);
#pragma unroll
      for (int e = 0; e < 4; ++e) st = dot2(xr[e], CH == 0 ? ones : xr[e], st);
      if (kk == 0 && do_cs) {
#pragma unroll
        for (int b = 0; b < 5; ++b) {
          const u32x4 wr = __builtin_bit_cast(u32x4, wf[b]);
#pragma unroll
          for (int e = 0; e < 4; ++e) cs[b] = dot2(wr[e], ones, cs[b]);
        }
      }
    }
    slot = slot == G1_NS - 1 ? 0 : slot + 1;
  }

  {
    lds_f32_ptr csp = (lds_f32_ptr)(smem + G1_CSP);
    lds_f32_ptr stat = (lds_f32_ptr)(smem + G1_STAT);
    st = swap_add(st);
    if (lane < 32) stat[CH * 128 + tb * 32 + lane] = st;
#pragma unroll
    for (int b = 0; b < 5; ++b) {
      const float v = swap_add(cs[b]);
      if (lane < 32) csp[tb * 320 + (CH * 5 + b) * 32 + lane] = v;
    }
    wait_lgkm0();
    bar();
    const int tid = w * 64 + lane;
    if (tid < 128) {
      const float mu = stat[tid] * (1.0f / DM);
      const float var = fmaxf(stat[128 + tid] * (1.0f / DM) - mu * mu, 0.f);
      const float rs = 1.0f / sqrtf(var + 1e-5f);
      ((lds_f32_ptr)(smem + G1_RS))[tid] = rs;
      ((lds_f32_ptr)(smem + G1_RM))[tid] = rs * mu;
    } else if (tid < 128 + 320) {
      const int c = tid - 128;
      ((lds_f32_ptr)(smem + G1_CS))[c] = (csp[c] + csp[320 + c]) + (csp[640 + c] + csp[960 + c]);
    }
    wait_lgkm0();
    bar();
  }
  const int tbg = pi * 4 + tb;
  const float rs = ((lds_f32_ptr)(smem + G1_RS))[tb * 32 + l31];
  const float rm = ((lds_f32_ptr)(smem + G1_RM))[tb * 32 + l31];
  auto fold_t = [&](const f32x16& a, int blk, float srs, float srm) {
    f32x16 o;
#pragma unroll
    for (int g = 0; g < 4; ++g) {
      const f32x4 c = ldsf4(smem + G1_CS + (blk * 32 + 8 * g + 4 * hi) * 4);
#pragma unroll
      for (int e = 0; e < 4; ++e) o[4 * g + e] = __builtin_fmaf(a[4 * g + e], srs, -srm * c[e]);
    }
    return o;
  };
  if (CH == 0) {
    const float qs = 0.25f * 1.4426950408889634f;
#pragma unroll
    for (int b = 0; b < 2; ++b) {
      const f32x16 v = b == 0 ? fold_t(acc[0], 0, rs * qs, rm * qs) : fold_t(acc[1], 1, rs, rm);
      char* dst = ws + (b == 0 ? WS_Q : WS_K) + ((size_t)(2 * cj) * NTB + tbg) * 1024 + lane * 16;
      *(bf16x8*)dst = pack8(v[0], v[1], v[2], v[3], v[4], v[5], v[6], v[7]);
      *(bf16x8*)(dst + NTB * 1024) = pack8(v[8], v[9], v[10], v[11], v[12], v[13], v[14], v[15]);
    }
    {
      const float c = ((lds_f32_ptr)(smem + G1_CS))[64 + l31];
      f32x16 v;
#pragma unroll
      for (int g = 0; g < 4; ++g) {
        const f32x4 a = ldsf4(smem + G1_RS + (tb * 32 + 8 * g + 4 * hi) * 4);
        const f32x4 m = ldsf4(smem + G1_RM + (tb * 32 + 8 * g + 4 * hi) * 4);
#pragma unroll
        for (int e = 0; e < 4; ++e) v[4 * g + e] = __builtin_fmaf(acc[2][4 * g + e], a[e], -m[e] * c);
      }
      char* dst = ws + WS_V + ((size_t)(2 * cj + (l31 >> 4)) * NTB + tbg) * VT + hi * 272;
      const int dd = l31 & 15;
      *(bf16x8*)(dst + 16 + 16 * dd) = pack8(v[0], v[1], v[2], v[3], v[4], v[5], v[6], v[7]);
      *(bf16x8*)(dst + 544 + 16 + 16 * dd) = pack8(v[8], v[9], v[10], v[11], v[12], v[13], v[14], v[15]);
      if (dd == 0) {
        const u32x4 o4 = {ones, ones, ones, ones};
        *(u32x4*)dst = o4;
        *(u32x4*)(dst + 544) = o4;
      }
    }
  }
#pragma unroll
  for (int b = (CH == 0 ? 3 : 0); b < 5; ++b) {
    const int gb = CH == 0 ? b - 3 : b + 2;
    const f32x16 v = fold_t(acc[b], CH * 5 + b, rs, rm);
    float h[8];
#pragma unroll
    for (int e = 0; e < 4; ++e) {
      h[e] = gelu_mul(v[e], v[4 + e]);
      h[4 + e] = gelu_mul(v[8 + e], v[12 + e]);
    }
    *(bf16x8*)(ws + WS_H + ((size_t)tbg * NKG + 7 * cj + gb) * 1024 + lane * 16) =
        pack8(h[0], h[1], h[2], h[3], h[4], h[5], h[6], h[7]);
  }
}

__global__ __launch_bounds__(512, 2) void k_gemm1(const char* x, const char* wqkv, const char* wg, const char* wu, char* ws) {
  __shared__ __attribute__((aligned(16))) char smem_raw[G1_LDS];
  lds_ptr smem = (lds_ptr)smem_raw;
  const int lane = threadIdx.x & 63, w = uniform(threadIdx.x >> 6);
  const int xcd = blockIdx.x & 7, loc = blockIdx.x >> 3;
  const int pi = (xcd & 1) * 8 + (loc & 7), cj = (xcd >> 1) * 4 + (loc >> 3);
  if (w < 4) g1_body<0>(smem, x, wqkv, wg, wu, ws, pi, cj, w, lane);
  else g1_body<1>(smem, x, wqkv, wg, wu, ws, pi, cj, w, lane);
}

constexpr int AT_K = 0, AT_V = 8192, AT_SLOT = 8192 + 8704, AT_NS = 3;
constexpr int AT_LDS = AT_NS * AT_SLOT;
constexpr float AT_MARGIN = 64.0f;

DEV void at_tile(bf16x8 kf, bf16x8 v0, bf16x8 v1, bf16x8 qf, const f32x16& negm, f32x16& o0, f32x16& o1) {
  const f32x16 s = mfma(kf, qf, negm);
  float p[16];
#pragma unroll
  for (int r = 0; r < 16; ++r) p[r] = __builtin_amdgcn_exp2f(s[r]);
  o0 = mfma(v0, pack8(p[0], p[1], p[2], p[3], p[4], p[5], p[6], p[7]), o0);
  o1 = mfma(v1, pack8(p[8], p[9], p[10], p[11], p[12], p[13], p[14], p[15]), o1);
}

DEV void at_true_max(const char* kg, const char* vg, bf16x8 qf, int lane, f32x16& o_out) {
  const int l31 = lane & 31, hi = lane >> 5;
  f32x16 zero, o0, o1;
#pragma unroll
  for (int r = 0; r < 16; ++r) zero[r] = 0.f;
  float m = -INFINITY;
  for (int t = 0; t < NTB; ++t) {
    const f32x16 s = mfma(*(const bf16x8*)(kg + t * 1024 + lane * 16), qf, zero);
#pragma unroll
    for (int r = 0; r < 16; ++r) m = fmaxf(m, s[r]);
  }
  m = swap_max(m);
  f32x16 negm;
#pragma unroll
  for (int r = 0; r < 16; ++r) negm[r] = -m;
  o0 = zero;
  o1 = zero;
  const int vo = hi * 272 + (l31 < 16 ? 16 + 16 * l31 : 0);
  for (int t = 0; t < NTB; ++t) {
    at_tile(*(const bf16x8*)(kg + t * 1024 + lane * 16), *(const bf16x8*)(vg + t * VT + vo),
            *(const bf16x8*)(vg + t * VT + 544 + vo), qf, negm, o0, o1);
  }
  o_out = o0 + o1;
}

__global__ __launch_bounds__(512, 2) void k_attn(char* ws) {
  __shared__ __attribute__((aligned(16))) char smem_raw[AT_LDS];
  lds_ptr smem = (lds_ptr)smem_raw;
  const int lane = threadIdx.x & 63, w = uniform(threadIdx.x >> 6);
  const int xcd = blockIdx.x & 7, loc = blockIdx.x >> 3;
  const int head = xcd * 4 + (loc & 3), qb = loc >> 2;
  const int l31 = lane & 31, hi = lane >> 5;
  const char* kg = ws + WS_K + (size_t)head * NTB * 1024;
  const char* vg = ws + WS_V + (size_t)head * NTB * VT;
  const uint32_t lbase = lds_addr(smem);
  const uint32_t dv = w * 1024 + lane * 16, dq = 8192 + w * 64 + lane * 16;
  auto issue = [&](int c, int slot) {
    const uint32_t so = lbase + slot * AT_SLOT;
    glds16(dv, kg + c * 8192, so + AT_K + w * 1024);
    glds16(dv, vg + c * (8 * VT), so + AT_V + w * 1024);
    glds16_q(dq, vg + c * (8 * VT), so + AT_V + 8192 + w * 64);
  };
  issue(0, 0);
  issue(1, 1);
  const int tq = qb * 8 + w;
  const bf16x8 qf = *(const bf16x8*)(ws + WS_Q + ((size_t)head * NTB + tq) * 1024 + lane * 16);
  const int ko = AT_K + lane * 16;
  const int vo = AT_V + hi * 272 + (l31 < 16 ? 16 + 16 * l31 : 0);
  f32x16 o0, o1, negm;
#pragma unroll
  for (int r = 0; r < 16; ++r) o0[r] = o1[r] = negm[r] = 0.f;
  int slot = 0;
  for (int c = 0; c < 8; ++c) {
    if (c < 7) wait_vm<3>();
    else wait_vm<0>();
    bar();
    if (c + 2 < 8) issue(c + 2, slot == 0 ? 2 : slot - 1);
    lds_ptr sb = smem + slot * AT_SLOT;
    if (c == 0) {
      const f32x16 s0 = mfma(as_frag(lds128(sb + ko)), qf, negm);
      const f32x16 s1 = mfma(as_frag(lds128(sb + ko + 1024)), qf, negm);
      float m = fmaxf(s0[0], s1[0]);
#pragma unroll
      for (int r = 1; r < 16; ++r) m = fmaxf(m, fmaxf(s0[r], s1[r]));
      m = swap_max(m) + AT_MARGIN;
#pragma unroll
      for (int r = 0; r < 16; ++r) negm[r] = -m;
    }
#pragma unroll
    for (int t = 0; t < 8; ++t)
      at_tile(as_frag(lds128(sb + ko + t * 1024)), as_frag(lds128(sb + vo + t * VT)), as_frag(lds128(sb + vo + t * VT + 544)), qf,
              negm, o0, o1);
    slot = slot == 2 ? 0 : slot + 1;
  }
  f32x16 o = o0 + o1;
  {
    bool ok = o[8] > 0x1p-100f && o[8] < 0x1p100f;
#pragma unroll
    for (int r = 0; r < 8; ++r) ok = ok && (fabsf(o[r]) < 0x1p120f);
    if (__builtin_amdgcn_ballot_w64(!ok) != 0) at_true_max(kg, vg, qf, lane, o);
  }
  const float inv = 1.0f / o[8];
  *(bf16x8*)(ws + WS_O + ((size_t)tq * 32 + head) * 1024 + lane * 16) =
      pack8(o[0] * inv, o[1] * inv, o[2] * inv, o[3] * inv, o[4] * inv, o[5] * inv, o[6] * inv, o[7] * inv);
}

constexpr int P3_STAGE = 32768, P3_NS = 4, P3_NSTAGE = 9;
constexpr int P3_LDS = P3_NS * P3_STAGE;

__global__ __launch_bounds__(512, 2) void k_out(const char* x, const char* wo, const char* wd, const char* ws, char* out) {
  __shared__ __attribute__((aligned(16))) char smem_raw[P3_LDS];
  lds_ptr smem = (lds_ptr)smem_raw;
  const int lane = threadIdx.x & 63, w = uniform(threadIdx.x >> 6);
  const int xcd = blockIdx.x & 7, loc = blockIdx.x >> 3;
  const int tp = xcd * 4 + (loc & 3), cblk = loc >> 2;
  const int tb = w & 1, kq = w >> 1, l31 = lane & 31, hi = lane >> 5;
  const int tbg = tp * 2 + tb;
  const uint32_t lbase = lds_addr(smem);
  const int drow = 8 * w + (lane >> 3);
  const uint32_t dvo = drow * 1024 + (((lane & 7) ^ (((drow >> 1) & 1) << 2)) << 4);
  auto issue = [&](int s, int slot) {
    const uint32_t so = lbase + slot * P3_STAGE + w * 1024;
#pragma unroll
    for (int q = 0; q < 4; ++q) {
      const char* src = s < 7 ? wd + (size_t)(q * 448 + s * 64) * 1024 : wo + (size_t)(q * 128 + (s - 7) * 64) * 1024;
      glds16(dvo, src + cblk * 128, so + q * 8192);
    }
  };
  auto afrag = [&](int s, int ks) -> u32x4 {
    const char* src = s < 7 ? ws + WS_H + ((size_t)tbg * NKG + kq * 28 + s * 4 + ks) * 1024
                            : ws + WS_O + ((size_t)tbg * 32 + kq * 8 + (s - 7) * 4 + ks) * 1024;
    return *(const u32x4*)(src + lane * 16);
  };
  u32x4 hf[2][4];
#pragma unroll
  for (int ks = 0; ks < 4; ++ks) hf[0][ks] = afrag(0, ks);
  issue(0, 0);
  issue(1, 1);
  issue(2, 2);
  const int g16 = (lane >> 4) & 1, q4 = (lane >> 2) & 3, p4 = lane & 3;
  int wo_[2];
#pragma unroll
  for (int cb = 0; cb < 2; ++cb) {
    const int row = 4 * hi + q4;
    wo_[cb] = kq * 8192 + row * 128 + ((cb ^ ((row >> 1) & 1)) << 6) + 32 * g16 + 8 * p4;
  }
  f32x16 accb[2], accs[2];
#pragma unroll
  for (int cb = 0; cb < 2; ++cb)
#pragma unroll
    for (int r = 0; r < 16; ++r) accb[cb][r] = accs[cb][r] = 0.f;
#pragma unroll
  for (int s = 0; s < P3_NSTAGE; ++s) {
    if (s + 1 < P3_NSTAGE) {
#pragma unroll
      for (int ks = 0; ks < 4; ++ks) hf[(s + 1) & 1][ks] = afrag(s + 1, ks);
    }
    if (s + 1 >= P3_NSTAGE) wait_vm<0>();
    else if (s + 2 >= P3_NSTAGE) wait_vm<4>();
    else if (s == 0) wait_vm<12>();
    else wait_vm<12>();
    bar();
    if (s + 3 < P3_NSTAGE) issue(s + 3, (s + 3) & 3);
    lds_ptr sb = smem + (s & 3) * P3_STAGE;
#pragma unroll
    for (int ks = 0; ks < 4; ++ks) {
      const bf16x8 bfr = as_frag(hf[s & 1][ks]);
#pragma unroll
      for (int cb = 0; cb < 2; ++cb) {
        const bf16x8 wf = lds_tr8(sb + wo_[cb] + ks * 2048, sb + wo_[cb] + ks * 2048 + 1024);
        if (s < 7) accb[cb] = mfma(wf, bfr, accb[cb]);
        else accs[cb] = mfma(wf, bfr, accs[cb]);
      }
    }
  }
  bar();
#pragma unroll
  for (int cb = 0; cb < 2; ++cb)
#pragma unroll
    for (int g = 0; g < 4; ++g) {
      const int o = (((tb * 2 + cb) * 4 + kq) * 4 + g) * 1024 + lane * 16;
      f32x4 a = {accb[cb][4 * g], accb[cb][4 * g + 1], accb[cb][4 * g + 2], accb[cb][4 * g + 3]};
      f32x4 b = {accs[cb][4 * g], accs[cb][4 * g + 1], accs[cb][4 * g + 2], accs[cb][4 * g + 3]};
      *(__attribute__((address_space(3))) f32x4*)(smem + o) = a;
      *(__attribute__((address_space(3))) f32x4*)(smem + 65536 + o) = b;
    }
  wait_lgkm0();
  bar();
  {
    const int cb = kq >> 1, g0 = 2 * (kq & 1);
    float res[8];
    const int tok = tbg * 32 + l31;
#pragma unroll
    for (int gg = 0; gg < 2; ++gg) {
      f32x4 b = {0.f, 0.f, 0.f, 0.f}, s = {0.f, 0.f, 0.f, 0.f};
#pragma unroll
      for (int k = 0; k < 4; ++k) {
        const int o = (((tb * 2 + cb) * 4 + k) * 4 + g0 + gg) * 1024 + lane * 16;
        b += ldsf4(smem + o);
        s += ldsf4(smem + 65536 + o);
      }
      const int col = cblk * 64 + cb * 32 + 8 * (g0 + gg) + 4 * hi;
      const u32x2 xr = *(const u32x2*)(x + ((size_t)tok * DM + col) * 2);
      const float xv[4] = {__uint_as_float(xr[0] << 16), __uint_as_float(xr[0] & 0xffff0000u), __uint_as_float(xr[1] << 16),
                           __uint_as_float(xr[1] & 0xffff0000u)};
#pragma unroll
      for (int e = 0; e < 4; ++e) {
        const float sig = __builtin_amdgcn_rcpf(1.0f + __builtin_amdgcn_exp2f(-1.4426950408889634f * b[e]));
        res[4 * gg + e] = __builtin_fmaf(s[e], b[e] * sig, xv[e]);
      }
      const u32x2 pk = {pack2(res[4 * gg], res[4 * gg + 1]), pack2(res[4 * gg + 2], res[4 * gg + 3])};
      *(u32x2*)(out + ((size_t)tok * DM + col) * 2) = pk;
    }
  }
}

extern "C" void kernel_launch(void* const* d_in, const int* in_sizes, int n_in, void* d_out, int out_size, void* d_ws,
                              size_t ws_size, hipStream_t stream) {
  const char* x = (const char*)d_in[0];
  const char* wqkv = (const char*)d_in[1];
  const char* wo = (const char*)d_in[2];
  const char* wg = (const char*)d_in[4];
  const char* wu = (const char*)d_in[5];
  const char* wd = (const char*)d_in[6];
  char* ws = (char*)d_ws;
  k_gemm1<<<256, 512, 0, stream>>>(x, wqkv, wg, wu, ws);
  k_attn<<<256, 512, 0, stream>>>(ws);
  k_out<<<256, 512, 0, stream>>>(x, wo, wd, ws, (char*)d_out);
}
```

```cpp
#include <hip/hip_runtime.h>
#include <stdint.h>
#define STAG 0
#define PRON 23
#define LPRIO 3

typedef __attribute__((ext_vector_type(8))) __bf16 bf16x8;
typedef __attribute__((ext_vector_type(2))) __bf16 bf16x2;
typedef __attribute__((ext_vector_type(16))) float f32x16;
typedef __attribute__((ext_vector_type(4))) float f32x4;
typedef __attribute__((ext_vector_type(2))) float f32x2;
typedef __attribute__((ext_vector_type(4))) unsigned u32x4;
typedef __attribute__((ext_vector_type(2))) unsigned u32x2;
typedef __attribute__((ext_vector_type(4))) short v4i16_t;
typedef __attribute__((address_space(3))) char* lds_ptr;
typedef __attribute__((address_space(3))) const u32x4* lds_u32x4_ptr;
typedef __attribute__((address_space(3))) const f32x4* lds_f32x4_ptr;
typedef __attribute__((address_space(3))) float* lds_f32_ptr;
typedef __attribute__((address_space(3))) v4i16_t* lds_tr_ptr;

#define DEV __device__ __forceinline__

constexpr int SEQ = 2048, DM = 512, DF = 1792;
constexpr int NTB = SEQ / 32;
constexpr int NKG = DF / 16;
constexpr int VT = 1088;
constexpr size_t WS_Q = 0;
constexpr size_t WS_K = WS_Q + 32u * NTB * 1024;
constexpr size_t WS_V = WS_K + 32u * NTB * 1024;
constexpr size_t WS_H = WS_V + 32u * NTB * VT + 4096;
constexpr size_t WS_O = WS_H + (size_t)NTB * NKG * 1024;
constexpr size_t WS_RSG = WS_O + (size_t)NTB * 32 * 1024;
constexpr size_t WS_CSG = WS_RSG + 16 * 128 * 16;
constexpr size_t WS_EP = WS_CSG + 16 * 2 * 320 * 8;
constexpr size_t WS_DUMMY = WS_EP + 256;
constexpr size_t WS_F = WS_DUMMY + 256 * 512;

DEV f32x16 mfma(bf16x8 a, bf16x8 b, f32x16 c) { return __builtin_amdgcn_mfma_f32_32x32x16_bf16(a, b, c, 0, 0, 0); }
DEV unsigned pack2(float lo, float hi) {
  bf16x2 r = {(__bf16)lo, (__bf16)hi};
  return __builtin_bit_cast(unsigned, r);
}
DEV bf16x8 as_frag(u32x4 v) { return __builtin_bit_cast(bf16x8, v); }
DEV bf16x8 pack8(float a0, float a1, float a2, float a3, float a4, float a5, float a6, float a7) {
  u32x4 r = {pack2(a0, a1), pack2(a2, a3), pack2(a4, a5), pack2(a6, a7)};
  return as_frag(r);
}
DEV u32x4 lds128(lds_ptr p) { return *(lds_u32x4_ptr)p; }
DEV f32x4 ldsf4(lds_ptr p) { return *(lds_f32x4_ptr)p; }
DEV bf16x8 lds_tr8(lds_ptr p0, lds_ptr p1) {
  v4i16_t a = __builtin_amdgcn_ds_read_tr16_b64_v4i16((lds_tr_ptr)p0);
  v4i16_t b = __builtin_amdgcn_ds_read_tr16_b64_v4i16((lds_tr_ptr)p1);
  u32x2 x = __builtin_bit_cast(u32x2, a), y = __builtin_bit_cast(u32x2, b);
  u32x4 r = {x[0], x[1], y[0], y[1]};
  return as_frag(r);
}
DEV void glds16(uint32_t voff, const void* sbase, uint32_t ldsaddr) {
  asm volatile("s_mov_b32 m0, %2\n\ts_nop 0\n\tglobal_load_lds_dwordx4 %0, %1" ::"v"(voff), "s"(sbase), "s"(ldsaddr) : "memory");
}
DEV void glds16_q(uint32_t voff, const void* sbase, uint32_t ldsaddr) {
  asm volatile("s_mov_b32 m0, %2\n\ts_mov_b64 exec, 15\n\tglobal_load_lds_dwordx4 %0, %1\n\ts_mov_b64 exec, -1" ::"v"(voff), "s"(sbase), "s"(ldsaddr) : "memory");
}
template <class T> DEV void st16w(char* p, T v) {
  asm volatile("global_store_dwordx4 %0, %1, off sc1\n\ts_nop 1" ::"v"(p), "v"(v) : "memory");
}
template <class T> DEV void st16p(char* p, T v) { *(T*)p = v; }
#define ST1 st16w
#define ST2 st16w
template <int N> DEV void wait_vm() { asm volatile("s_waitcnt vmcnt(%0)" ::"n"(N) : "memory"); }
DEV void bar() { asm volatile("s_waitcnt lgkmcnt(0)\n\ts_barrier" ::: "memory"); }
DEV void wait_lgkm0() { asm volatile("s_waitcnt lgkmcnt(0)" ::: "memory"); }
DEV float swap_add(float v) {
  auto r = __builtin_amdgcn_permlane32_swap(__float_as_uint(v), __float_as_uint(v), false, false);
  return __uint_as_float(r[0]) + __uint_as_float(r[1]);
}
DEV float swap_max(float v) {
  auto r = __builtin_amdgcn_permlane32_swap(__float_as_uint(v), __float_as_uint(v), false, false);
  return fmaxf(__uint_as_float(r[0]), __uint_as_float(r[1]));
}
DEV float dot2(unsigned a, unsigned b, float c) {
  return __builtin_amdgcn_fdot2_f32_bf16(__builtin_bit_cast(bf16x2, a), __builtin_bit_cast(bf16x2, b), c, false);
}
DEV uint32_t lds_addr(lds_ptr p) { return (uint32_t)(uintptr_t)p; }
DEV int uniform(int v) { return __builtin_amdgcn_readfirstlane(v); }

#define G1_LOOP_X \
  "s_barrier\n\t" \
  "ds_read_b128 v[80:83], v138 offset:0\n\t" \
  "ds_read_b64_tr_b16 v[84:85], v128 offset:0\n\t" \
  "ds_read_b64_tr_b16 v[86:87], v128 offset:1024\n\t" \
  "ds_read_b64_tr_b16 v[88:89], v129 offset:0\n\t" \
  "ds_read_b64_tr_b16 v[90:91], v129 offset:1024\n\t" \
  "ds_read_b64_tr_b16 v[92:93], v130 offset:0\n\t" \
  "ds_read_b64_tr_b16 v[94:95], v130 offset:1024\n\t" \
  "ds_read_b64_tr_b16 v[96:97], v131 offset:0\n\t" \
  "ds_read_b64_tr_b16 v[98:99], v131 offset:1024\n\t" \
  "ds_read_b64_tr_b16 v[100:101], v132 offset:0\n\t" \
  "ds_read_b64_tr_b16 v[102:103], v132 offset:512\n\t" \
  "s_waitcnt lgkmcnt(6)\n\t" \
  "v_mfma_f32_32x32x16_bf16 v[0:15], v[84:87], v[80:83], v[0:15]\n\t" \
  "ds_read_b128 v[104:107], v139 offset:0\n\t" \
  "ds_read_b64_tr_b16 v[108:109], v128 offset:4096\n\t" \
  "ds_read_b64_tr_b16 v[110:111], v128 offset:5120\n\t" \
  "v_mfma_f32_32x32x16_bf16 v[16:31], v[88:91], v[80:83], v[16:31]\n\t" \
  "ds_read_b64_tr_b16 v[112:113], v129 offset:4096\n\t" \
  "ds_read_b64_tr_b16 v[114:115], v129 offset:5120\n\t" \
  "s_waitcnt lgkmcnt(7)\n\t" \
  "v_mfma_f32_32x32x16_bf16 v[32:47], v[92:95], v[80:83], v[32:47]\n\t" \
  "ds_read_b64_tr_b16 v[116:117], v130 offset:4096\n\t" \
  "ds_read_b64_tr_b16 v[118:119], v130 offset:5120\n\t" \
  "v_mfma_f32_32x32x16_bf16 v[48:63], v[96:99], v[80:83], v[48:63]\n\t" \
  "ds_read_b64_tr_b16 v[120:121], v131 offset:4096\n\t" \
  "ds_read_b64_tr_b16 v[122:123], v131 offset:5120\n\t" \
  "s_waitcnt lgkmcnt(9)\n\t" \
  "v_mfma_f32_32x32x16_bf16 v[64:79], v[100:103], v[80:83], v[64:79]\n\t" \
  "ds_read_b64_tr_b16 v[124:125], v132 offset:2048\n\t" \
  "ds_read_b64_tr_b16 v[126:127], v132 offset:2560\n\t" \
  "s_waitcnt lgkmcnt(6)\n\t" \
  "v_mfma_f32_32x32x16_bf16 v[0:15], v[108:111], v[104:107], v[0:15]\n\t" \
  "s_barrier\n\t" \
  "ds_read_b128 v[80:83], v140 offset:0\n\t" \
  "ds_read_b64_tr_b16 v[84:85], v128 offset:20480\n\t" \
  "ds_read_b64_tr_b16 v[86:87], v128 offset:21504\n\t" \
  "v_mfma_f32_32x32x16_bf16 v[16:31], v[112:115], v[104:107], v[16:31]\n\t" \
  "ds_read_b64_tr_b16 v[88:89], v129 offset:20480\n\t" \
  "ds_read_b64_tr_b16 v[90:91], v129 offset:21504\n\t" \
  "s_waitcnt lgkmcnt(7)\n\t" \
  "v_mfma_f32_32x32x16_bf16 v[32:47], v[116:119], v[104:107], v[32:47]\n\t" \
  "ds_read_b64_tr_b16 v[92:93], v130 offset:20480\n\t" \
  "ds_read_b64_tr_b16 v[94:95], v130 offset:21504\n\t" \
  "v_mfma_f32_32x32x16_bf16 v[48:63], v[120:123], v[104:107], v[48:63]\n\t" \
  "ds_read_b64_tr_b16 v[96:97], v131 offset:20480\n\t" \
  "ds_read_b64_tr_b16 v[98:99], v131 offset:21504\n\t" \
  "s_waitcnt lgkmcnt(9)\n\t" \
  "v_mfma_f32_32x32x16_bf16 v[64:79], v[124:127], v[104:107], v[64:79]\n\t" \
  "ds_read_b64_tr_b16 v[100:101], v132 offset:20480\n\t" \
  "ds_read_b64_tr_b16 v[102:103], v132 offset:20992\n\t" \
  "s_waitcnt lgkmcnt(6)\n\t" \
  "v_mfma_f32_32x32x16_bf16 v[0:15], v[84:87], v[80:83], v[0:15]\n\t" \
  "ds_read_b128 v[104:107], v141 offset:0\n\t" \
  "ds_read_b64_tr_b16 v[108:109], v128 offset:24576\n\t" \
  "ds_read_b64_tr_b16 v[110:111], v128 offset:25600\n\t" \
  "v_mfma_f32_32x32x16_bf16 v[16:31], v[88:91], v[80:83], v[16:31]\n\t" \
  "ds_read_b64_tr_b16 v[112:113], v129 offset:24576\n\t" \
  "ds_read_b64_tr_b16 v[114:115], v129 offset:25600\n\t" \
  "s_waitcnt lgkmcnt(7)\n\t" \
  "v_mfma_f32_32x32x16_bf16 v[32:47], v[92:95], v[80:83], v[32:47]\n\t" \
  "ds_read_b64_tr_b16 v[116:117], v130 offset:24576\n\t" \
  "ds_read_b64_tr_b16 v[118:119], v130 offset:25600\n\t" \
  "v_mfma_f32_32x32x16_bf16 v[48:63], v[96:99], v[80:83], v[48:63]\n\t" \
  "ds_read_b64_tr_b16 v[120:121], v131 offset:24576\n\t" \
  "ds_read_b64_tr_b16 v[122:123], v131 offset:25600\n\t" \
  "s_waitcnt lgkmcnt(9)\n\t" \
  "v_mfma_f32_32x32x16_bf16 v[64:79], v[100:103], v[80:83], v[64:79]\n\t" \
  "ds_read_b64_tr_b16 v[124:125], v132 offset:22528\n\t" \
  "ds_read_b64_tr_b16 v[126:127], v132 offset:23040\n\t" \
  "s_waitcnt lgkmcnt(6)\n\t" \
  "v_mfma_f32_32x32x16_bf16 v[0:15], v[108:111], v[104:107], v[0:15]\n\t" \
  "s_barrier\n\t" \
  "ds_read_b128 v[80:83], v138 offset:16384\n\t" \
  "ds_read_b64_tr_b16 v[84:85], v128 offset:40960\n\t" \
  "ds_read_b64_tr_b16 v[86:87], v128 offset:41984\n\t" \
  "v_mfma_f32_32x32x16_bf16 v[16:31], v[112:115], v[104:107], v[16:31]\n\t" \
  "ds_read_b64_tr_b16 v[88:89], v129 offset:40960\n\t" \
  "ds_read_b64_tr_b16 v[90:91], v129 offset:41984\n\t" \
  "s_waitcnt lgkmcnt(7)\n\t" \
  "v_mfma_f32_32x32x16_bf16 v[32:47], v[116:119], v[104:107], v[32:47]\n\t" \
  "ds_read_b64_tr_b16 v[92:93], v130 offset:40960\n\t" \
  "ds_read_b64_tr_b16 v[94:95], v130 offset:41984\n\t" \
  "v_mfma_f32_32x32x16_bf16 v[48:63], v[120:123], v[104:107], v[48:63]\n\t" \
  "ds_read_b64_tr_b16 v[96:97], v131 offset:40960\n\t" \
  "ds_read_b64_tr_b16 v[98:99], v131 offset:41984\n\t" \
  "s_waitcnt lgkmcnt(9)\n\t" \
  "v_mfma_f32_32x32x16_bf16 v[64:79], v[124:127], v[104:107], v[64:79]\n\t" \
  "ds_read_b64_tr_b16 v[100:101], v132 offset:40960\n\t" \
  "ds_read_b64_tr_b16 v[102:103], v132 offset:41472\n\t" \
  "s_waitcnt lgkmcnt(6)\n\t" \
  "v_mfma_f32_32x32x16_bf16 v[0:15], v[84:87], v[80:83], v[0:15]\n\t" \
  "ds_read_b128 v[104:107], v139 offset:16384\n\t" \
  "ds_read_b64_tr_b16 v[108:109], v128 offset:45056\n\t" \
  "ds_read_b64_tr_b16 v[110:111], v128 offset:46080\n\t" \
  "v_mfma_f32_32x32x16_bf16 v[16:31], v[88:91], v[80:83], v[16:31]\n\t" \
  "ds_read_b64_tr_b16 v[112:113], v129 offset:45056\n\t" \
  "ds_read_b64_tr_b16 v[114:115], v129 offset:46080\n\t" \
  "s_waitcnt lgkmcnt(7)\n\t" \
  "v_mfma_f32_32x32x16_bf16 v[32:47], v[92:95], v[80:83], v[32:47]\n\t" \
  "ds_read_b64_tr_b16 v[116:117], v130 offset:45056\n\t" \
  "ds_read_b64_tr_b16 v[118:119], v130 offset:46080\n\t" \
  "v_mfma_f32_32x32x16_bf16 v[48:63], v[96:99], v[80:83], v[48:63]\n\t" \
  "ds_read_b64_tr_b16 v[120:121], v131 offset:45056\n\t" \
  "ds_read_b64_tr_b16 v[122:123], v131 offset:46080\n\t" \
  "s_waitcnt lgkmcnt(9)\n\t" \
  "v_mfma_f32_32x32x16_bf16 v[64:79], v[100:103], v[80:83], v[64:79]\n\t" \
  "ds_read_b64_tr_b16 v[124:125], v132 offset:43008\n\t" \
  "ds_read_b64_tr_b16 v[126:127], v132 offset:43520\n\t" \
  "s_waitcnt lgkmcnt(6)\n\t" \
  "v_mfma_f32_32x32x16_bf16 v[0:15], v[108:111], v[104:107], v[0:15]\n\t" \
  "s_barrier\n\t" \
  "ds_read_b128 v[80:83], v140 offset:16384\n\t" \
  "ds_read_b64_tr_b16 v[84:85], v133 offset:0\n\t" \
  "ds_read_b64_tr_b16 v[86:87], v133 offset:1024\n\t" \
  "v_mfma_f32_32x32x16_bf16 v[16:31], v[112:115], v[104:107], v[16:31]\n\t" \
  "ds_read_b64_tr_b16 v[88:89], v134 offset:0\n\t" \
  "ds_read_b64_tr_b16 v[90:91], v134 offset:1024\n\t" \
  "s_waitcnt lgkmcnt(7)\n\t" \
  "v_mfma_f32_32x32x16_bf16 v[32:47], v[116:119], v[104:107], v[32:47]\n\t" \
  "ds_read_b64_tr_b16 v[92:93], v135 offset:0\n\t" \
  "ds_read_b64_tr_b16 v[94:95], v135 offset:1024\n\t" \
  "v_mfma_f32_32x32x16_bf16 v[48:63], v[120:123], v[104:107], v[48:63]\n\t" \
  "ds_read_b64_tr_b16 v[96:97], v136 offset:0\n\t" \
  "ds_read_b64_tr_b16 v[98:99], v136 offset:1024\n\t" \
  "s_waitcnt lgkmcnt(9)\n\t" \
  "v_mfma_f32_32x32x16_bf16 v[64:79], v[124:127], v[104:107], v[64:79]\n\t" \
  "ds_read_b64_tr_b16 v[100:101], v137 offset:0\n\t" \
  "ds_read_b64_tr_b16 v[102:103], v137 offset:512\n\t" \
  "s_waitcnt lgkmcnt(6)\n\t" \
  "v_mfma_f32_32x32x16_bf16 v[0:15], v[84:87], v[80:83], v[0:15]\n\t" \
  "ds_read_b128 v[104:107], v141 offset:16384\n\t" \
  "ds_read_b64_tr_b16 v[108:109], v133 offset:4096\n\t" \
  "ds_read_b64_tr_b16 v[110:111], v133 offset:5120\n\t" \
  "v_mfma_f32_32x32x16_bf16 v[16:31], v[88:91], v[80:83], v[16:31]\n\t" \
  "ds_read_b64_tr_b16 v[112:113], v134 offset:4096\n\t" \
  "ds_read_b64_tr_b16 v[114:115], v134 offset:5120\n\t" \
  "s_waitcnt lgkmcnt(7)\n\t" \
  "v_mfma_f32_32x32x16_bf16 v[32:47], v[92:95], v[80:83], v[32:47]\n\t" \
  "ds_read_b64_tr_b16 v[116:117], v135 offset:4096\n\t" \
  "ds_read_b64_tr_b16 v[118:119], v135 offset:5120\n\t" \
  "v_mfma_f32_32x32x16_bf16 v[48:63], v[96:99], v[80:83], v[48:63]\n\t" \
  "ds_read_b64_tr_b16 v[120:121], v136 offset:4096\n\t" \
  "ds_read_b64_tr_b16 v[122:123], v136 offset:5120\n\t" \
  "s_waitcnt lgkmcnt(9)\n\t" \
  "v_mfma_f32_32x32x16_bf16 v[64:79], v[100:103], v[80:83], v[64:79]\n\t" \
  "ds_read_b64_tr_b16 v[124:125], v137 offset:2048\n\t" \
  "ds_read_b64_tr_b16 v[126:127], v137 offset:2560\n\t" \
  "s_waitcnt lgkmcnt(6)\n\t" \
  "v_mfma_f32_32x32x16_bf16 v[0:15], v[108:111], v[104:107], v[0:15]\n\t" \
  "s_barrier\n\t" \
  "ds_read_b128 v[80:83], v138 offset:32768\n\t" \
  "ds_read_b64_tr_b16 v[84:85], v133 offset:20480\n\t" \
  "ds_read_b64_tr_b16 v[86:87], v133 offset:21504\n\t" \
  "v_mfma_f32_32x32x16_bf16 v[16:31], v[112:115], v[104:107], v[16:31]\n\t" \
  "ds_read_b64_tr_b16 v[88:89], v134 offset:20480\n\t" \
  "ds_read_b64_tr_b16 v[90:91], v134 offset:21504\n\t" \
  "s_waitcnt lgkmcnt(7)\n\t" \
  "v_mfma_f32_32x32x16_bf16 v[32:47], v[116:119], v[104:107], v[32:47]\n\t" \
  "ds_read_b64_tr_b16 v[92:93], v135 offset:20480\n\t" \
  "ds_read_b64_tr_b16 v[94:95], v135 offset:21504\n\t" \
  "v_mfma_f32_32x32x16_bf16 v[48:63], v[120:123], v[104:107], v[48:63]\n\t" \
  "ds_read_b64_tr_b16 v[96:97], v136 offset:20480\n\t" \
  "ds_read_b64_tr_b16 v[98:99], v136 offset:21504\n\t" \
  "s_waitcnt lgkmcnt(9)\n\t" \
  "v_mfma_f32_32x32x16_bf16 v[64:79], v[124:127], v[104:107], v[64:79]\n\t" \
  "ds_read_b64_tr_b16 v[100:101], v137 offset:20480\n\t" \
  "ds_read_b64_tr_b16 v[102:103], v137 offset:20992\n\t" \
  "s_waitcnt lgkmcnt(6)\n\t" \
  "v_mfma_f32_32x32x16_bf16 v[0:15], v[84:87], v[80:83], v[0:15]\n\t" \
  "ds_read_b128 v[104:107], v139 offset:32768\n\t" \
  "ds_read_b64_tr_b16 v[108:109], v133 offset:24576\n\t" \
  "ds_read_b64_tr_b16 v[110:111], v133 offset:25600\n\t" \
  "v_mfma_f32_32x32x16_bf16 v[16:31], v[88:91], v[80:83], v[16:31]\n\t" \
  "ds_read_b64_tr_b16 v[112:113], v134 offset:24576\n\t" \
  "ds_read_b64_tr_b16 v[114:115], v134 offset:25600\n\t" \
  "s_waitcnt lgkmcnt(7)\n\t" \
  "v_mfma_f32_32x32x16_bf16 v[32:47], v[92:95], v[80:83], v[32:47]\n\t" \
  "ds_read_b64_tr_b16 v[116:117], v135 offset:24576\n\t" \
  "ds_read_b64_tr_b16 v[118:119], v135 offset:25600\n\t" \
  "v_mfma_f32_32x32x16_bf16 v[48:63], v[96:99], v[80:83], v[48:63]\n\t" \
  "ds_read_b64_tr_b16 v[120:121], v136 offset:24576\n\t" \
  "ds_read_b64_tr_b16 v[122:123], v136 offset:25600\n\t" \
  "s_waitcnt lgkmcnt(9)\n\t" \
  "v_mfma_f32_32x32x16_bf16 v[64:79], v[100:103], v[80:83], v[64:79]\n\t" \
  "ds_read_b64_tr_b16 v[124:125], v137 offset:22528\n\t" \
  "ds_read_b64_tr_b16 v[126:127], v137 offset:23040\n\t" \
  "s_waitcnt lgkmcnt(6)\n\t" \
  "v_mfma_f32_32x32x16_bf16 v[0:15], v[108:111], v[104:107], v[0:15]\n\t" \
  "s_barrier\n\t" \
  "ds_read_b128 v[80:83], v140 offset:32768\n\t" \
  "ds_read_b64_tr_b16 v[84:85], v128 offset:0\n\t" \
  "ds_read_b64_tr_b16 v[86:87], v128 offset:1024\n\t" \
  "v_mfma_f32_32x32x16_bf16 v[16:31], v[112:115], v[104:107], v[16:31]\n\t" \
  "ds_read_b64_tr_b16 v[88:89], v129 offset:0\n\t" \
  "ds_read_b64_tr_b16 v[90:91], v129 offset:1024\n\t" \
  "s_waitcnt lgkmcnt(7)\n\t" \
  "v_mfma_f32_32x32x16_bf16 v[32:47], v[116:119], v[104:107], v[32:47]\n\t" \
  "ds_read_b64_tr_b16 v[92:93], v130 offset:0\n\t" \
  "ds_read_b64_tr_b16 v[94:95], v130 offset:1024\n\t" \
  "v_mfma_f32_32x32x16_bf16 v[48:63], v[120:123], v[104:107], v[48:63]\n\t" \
  "ds_read_b64_tr_b16 v[96:97], v131 offset:0\n\t" \
  "ds_read_b64_tr_b16 v[98:99], v131 offset:1024\n\t" \
  "s_waitcnt lgkmcnt(9)\n\t" \
  "v_mfma_f32_32x32x16_bf16 v[64:79], v[124:127], v[104:107], v[64:79]\n\t" \
  "ds_read_b64_tr_b16 v[100:101], v132 offset:0\n\t" \
  "ds_read_b64_tr_b16 v[102:103], v132 offset:512\n\t" \
  "s_waitcnt lgkmcnt(6)\n\t" \
  "v_mfma_f32_32x32x16_bf16 v[0:15], v[84:87], v[80:83], v[0:15]\n\t" \
  "ds_read_b128 v[104:107], v141 offset:32768\n\t" \
  "ds_read_b64_tr_b16 v[108:109], v128 offset:4096\n\t" \
  "ds_read_b64_tr_b16 v[110:111], v128 offset:5120\n\t" \
  "v_mfma_f32_32x32x16_bf16 v[16:31], v[88:91], v[80:83], v[16:31]\n\t" \
  "ds_read_b64_tr_b16 v[112:113], v129 offset:4096\n\t" \
  "ds_read_b64_tr_b16 v[114:115], v129 offset:5120\n\t" \
  "s_waitcnt lgkmcnt(7)\n\t" \
  "v_mfma_f32_32x32x16_bf16 v[32:47], v[92:95], v[80:83], v[32:47]\n\t" \
  "ds_read_b64_tr_b16 v[116:117], v130 offset:4096\n\t" \
  "ds_read_b64_tr_b16 v[118:119], v130 offset:5120\n\t" \
  "v_mfma_f32_32x32x16_bf16 v[48:63], v[96:99], v[80:83], v[48:63]\n\t" \
  "ds_read_b64_tr_b16 v[120:121], v131 offset:4096\n\t" \
  "ds_read_b64_tr_b16 v[122:123], v131 offset:5120\n\t" \
  "s_waitcnt lgkmcnt(9)\n\t" \
  "v_mfma_f32_32x32x16_bf16 v[64:79], v[100:103], v[80:83], v[64:79]\n\t" \
  "ds_read_b64_tr_b16 v[124:125], v132 offset:2048\n\t" \
  "ds_read_b64_tr_b16 v[126:127], v132 offset:2560\n\t" \
  "s_waitcnt lgkmcnt(6)\n\t" \
  "v_mfma_f32_32x32x16_bf16 v[0:15], v[108:111], v[104:107], v[0:15]\n\t" \
  "s_barrier\n\t" \
  "ds_read_b128 v[80:83], v138 offset:0\n\t" \
  "ds_read_b64_tr_b16 v[84:85], v128 offset:20480\n\t" \
  "ds_read_b64_tr_b16 v[86:87], v128 offset:21504\n\t" \
  "v_mfma_f32_32x32x16_bf16 v[16:31], v[112:115], v[104:107], v[16:31]\n\t" \
  "ds_read_b64_tr_b16 v[88:89], v129 offset:20480\n\t" \
  "ds_read_b64_tr_b16 v[90:91], v129 offset:21504\n\t" \
  "s_waitcnt lgkmcnt(7)\n\t" \
  "v_mfma_f32_32x32x16_bf16 v[32:47], v[116:119], v[104:107], v[32:47]\n\t" \
  "ds_read_b64_tr_b16 v[92:93], v130 offset:20480\n\t" \
  "ds_read_b64_tr_b16 v[94:95], v130 offset:21504\n\t" \
  "v_mfma_f32_32x32x16_bf16 v[48:63], v[120:123], v[104:107], v[48:63]\n\t" \
  "ds_read_b64_tr_b16 v[96:97], v131 offset:20480\n\t" \
  "ds_read_b64_tr_b16 v[98:99], v131 offset:21504\n\t" \
  "s_waitcnt lgkmcnt(9)\n\t" \
  "v_mfma_f32_32x32x16_bf16 v[64:79], v[124:127], v[104:107], v[64:79]\n\t" \
  "ds_read_b64_tr_b16 v[100:101], v132 offset:20480\n\t" \
  "ds_read_b64_tr_b16 v[102:103], v132 offset:20992\n\t" \
  "s_waitcnt lgkmcnt(6)\n\t" \
  "v_mfma_f32_32x32x16_bf16 v[0:15], v[84:87], v[80:83], v[0:15]\n\t" \
  "ds_read_b128 v[104:107], v139 offset:0\n\t" \
  "ds_read_b64_tr_b16 v[108:109], v128 offset:24576\n\t" \
  "ds_read_b64_tr_b16 v[110:111], v128 offset:25600\n\t" \
  "v_mfma_f32_32x32x16_bf16 v[16:31], v[88:91], v[80:83], v[16:31]\n\t" \
  "ds_read_b64_tr_b16 v[112:113], v129 offset:24576\n\t" \
  "ds_read_b64_tr_b16 v[114:115], v129 offset:25600\n\t" \
  "s_waitcnt lgkmcnt(7)\n\t" \
  "v_mfma_f32_32x32x16_bf16 v[32:47], v[92:95], v[80:83], v[32:47]\n\t" \
  "ds_read_b64_tr_b16 v[116:117], v130 offset:24576\n\t" \
  "ds_read_b64_tr_b16 v[118:119], v130 offset:25600\n\t" \
  "v_mfma_f32_32x32x16_bf16 v[48:63], v[96:99], v[80:83], v[48:63]\n\t" \
  "ds_read_b64_tr_b16 v[120:121], v131 offset:24576\n\t" \
  "ds_read_b64_tr_b16 v[122:123], v131 offset:25600\n\t" \
  "s_waitcnt lgkmcnt(9)\n\t" \
  "v_mfma_f32_32x32x16_bf16 v[64:79], v[100:103], v[80:83], v[64:79]\n\t" \
  "ds_read_b64_tr_b16 v[124:125], v132 offset:22528\n\t" \
  "ds_read_b64_tr_b16 v[126:127], v132 offset:23040\n\t" \
  "s_waitcnt lgkmcnt(6)\n\t" \
  "v_mfma_f32_32x32x16_bf16 v[0:15], v[108:111], v[104:107], v[0:15]\n\t" \
  "s_barrier\n\t" \
  "ds_read_b128 v[80:83], v140 offset:0\n\t" \
  "ds_read_b64_tr_b16 v[84:85], v128 offset:40960\n\t" \
  "ds_read_b64_tr_b16 v[86:87], v128 offset:41984\n\t" \
  "v_mfma_f32_32x32x16_bf16 v[16:31], v[112:115], v[104:107], v[16:31]\n\t" \
  "ds_read_b64_tr_b16 v[88:89], v129 offset:40960\n\t" \
  "ds_read_b64_tr_b16 v[90:91], v129 offset:41984\n\t" \
  "s_waitcnt lgkmcnt(7)\n\t" \
  "v_mfma_f32_32x32x16_bf16 v[32:47], v[116:119], v[104:107], v[32:47]\n\t" \
  "ds_read_b64_tr_b16 v[92:93], v130 offset:40960\n\t" \
  "ds_read_b64_tr_b16 v[94:95], v130 offset:41984\n\t" \
  "v_mfma_f32_32x32x16_bf16 v[48:63], v[120:123], v[104:107], v[48:63]\n\t" \
  "ds_read_b64_tr_b16 v[96:97], v131 offset:40960\n\t" \
  "ds_read_b64_tr_b16 v[98:99], v131 offset:41984\n\t" \
  "s_waitcnt lgkmcnt(9)\n\t" \
  "v_mfma_f32_32x32x16_bf16 v[64:79], v[124:127], v[104:107], v[64:79]\n\t" \
  "ds_read_b64_tr_b16 v[100:101], v132 offset:40960\n\t" \
  "ds_read_b64_tr_b16 v[102:103], v132 offset:41472\n\t" \
  "s_waitcnt lgkmcnt(6)\n\t" \
  "v_mfma_f32_32x32x16_bf16 v[0:15], v[84:87], v[80:83], v[0:15]\n\t" \
  "ds_read_b128 v[104:107], v141 offset:0\n\t" \
  "ds_read_b64_tr_b16 v[108:109], v128 offset:45056\n\t" \
  "ds_read_b64_tr_b16 v[110:111], v128 offset:46080\n\t" \
  "v_mfma_f32_32x32x16_bf16 v[16:31], v[88:91], v[80:83], v[16:31]\n\t" \
  "ds_read_b64_tr_b16 v[112:113], v129 offset:45056\n\t" \
  "ds_read_b64_tr_b16 v[114:115], v129 offset:46080\n\t" \
  "s_waitcnt lgkmcnt(7)\n\t" \
  "v_mfma_f32_32x32x16_bf16 v[32:47], v[92:95], v[80:83], v[32:47]\n\t" \
  "ds_read_b64_tr_b16 v[116:117], v130 offset:45056\n\t" \
  "ds_read_b64_tr_b16 v[118:119], v130 offset:46080\n\t" \
  "v_mfma_f32_32x32x16_bf16 v[48:63], v[96:99], v[80:83], v[48:63]\n\t" \
  "ds_read_b64_tr_b16 v[120:121], v131 offset:45056\n\t" \
  "ds_read_b64_tr_b16 v[122:123], v131 offset:46080\n\t" \
  "s_waitcnt lgkmcnt(9)\n\t" \
  "v_mfma_f32_32x32x16_bf16 v[64:79], v[100:103], v[80:83], v[64:79]\n\t" \
  "ds_read_b64_tr_b16 v[124:125], v132 offset:43008\n\t" \
  "ds_read_b64_tr_b16 v[126:127], v132 offset:43520\n\t" \
  "s_waitcnt lgkmcnt(6)\n\t" \
  "v_mfma_f32_32x32x16_bf16 v[0:15], v[108:111], v[104:107], v[0:15]\n\t" \
  "s_barrier\n\t" \
  "ds_read_b128 v[80:83], v138 offset:16384\n\t" \
  "ds_read_b64_tr_b16 v[84:85], v133 offset:0\n\t" \
  "ds_read_b64_tr_b16 v[86:87], v133 offset:1024\n\t" \
  "v_mfma_f32_32x32x16_bf16 v[16:31], v[112:115], v[104:107], v[16:31]\n\t" \
  "ds_read_b64_tr_b16 v[88:89], v134 offset:0\n\t" \
  "ds_read_b64_tr_b16 v[90:91], v134 offset:1024\n\t" \
  "s_waitcnt lgkmcnt(7)\n\t" \
  "v_mfma_f32_32x32x16_bf16 v[32:47], v[116:119], v[104:107], v[32:47]\n\t" \
  "ds_read_b64_tr_b16 v[92:93], v135 offset:0\n\t" \
  "ds_read_b64_tr_b16 v[94:95], v135 offset:1024\n\t" \
  "v_mfma_f32_32x32x16_bf16 v[48:63], v[120:123], v[104:107], v[48:63]\n\t" \
  "ds_read_b64_tr_b16 v[96:97], v136 offset:0\n\t" \
  "ds_read_b64_tr_b16 v[98:99], v136 offset:1024\n\t" \
  "s_waitcnt lgkmcnt(9)\n\t" \
  "v_mfma_f32_32x32x16_bf16 v[64:79], v[124:127], v[104:107], v[64:79]\n\t" \
  "ds_read_b64_tr_b16 v[100:101], v137 offset:0\n\t" \
  "ds_read_b64_tr_b16 v[102:103], v137 offset:512\n\t" \
  "s_waitcnt lgkmcnt(6)\n\t" \
  "v_mfma_f32_32x32x16_bf16 v[0:15], v[84:87], v[80:83], v[0:15]\n\t" \
  "ds_read_b128 v[104:107], v139 offset:16384\n\t" \
  "ds_read_b64_tr_b16 v[108:109], v133 offset:4096\n\t" \
  "ds_read_b64_tr_b16 v[110:111], v133 offset:5120\n\t" \
  "v_mfma_f32_32x32x16_bf16 v[16:31], v[88:91], v[80:83], v[16:31]\n\t" \
  "ds_read_b64_tr_b16 v[112:113], v134 offset:4096\n\t" \
  "ds_read_b64_tr_b16 v[114:115], v134 offset:5120\n\t" \
  "s_waitcnt lgkmcnt(7)\n\t" \
  "v_mfma_f32_32x32x16_bf16 v[32:47], v[92:95], v[80:83], v[32:47]\n\t" \
  "ds_read_b64_tr_b16 v[116:117], v135 offset:4096\n\t" \
  "ds_read_b64_tr_b16 v[118:119], v135 offset:5120\n\t" \
  "v_mfma_f32_32x32x16_bf16 v[48:63], v[96:99], v[80:83], v[48:63]\n\t" \
  "ds_read_b64_tr_b16 v[120:121], v136 offset:4096\n\t" \
  "ds_read_b64_tr_b16 v[122:123], v136 offset:5120\n\t" \
  "s_waitcnt lgkmcnt(9)\n\t" \
  "v_mfma_f32_32x32x16_bf16 v[64:79], v[100:103], v[80:83], v[64:79]\n\t" \
  "ds_read_b64_tr_b16 v[124:125], v137 offset:2048\n\t" \
  "ds_read_b64_tr_b16 v[126:127], v137 offset:2560\n\t" \
  "s_waitcnt lgkmcnt(6)\n\t" \
  "v_mfma_f32_32x32x16_bf16 v[0:15], v[108:111], v[104:107], v[0:15]\n\t" \
  "s_barrier\n\t" \
  "ds_read_b128 v[80:83], v140 offset:16384\n\t" \
  "ds_read_b64_tr_b16 v[84:85], v133 offset:20480\n\t" \
  "ds_read_b64_tr_b16 v[86:87], v133 offset:21504\n\t" \
  "v_mfma_f32_32x32x16_bf16 v[16:31], v[112:115], v[104:107], v[16:31]\n\t" \
  "ds_read_b64_tr_b16 v[88:89], v134 offset:20480\n\t" \
  "ds_read_b64_tr_b16 v[90:91], v134 offset:21504\n\t" \
  "s_waitcnt lgkmcnt(7)\n\t" \
  "v_mfma_f32_32x32x16_bf16 v[32:47], v[116:119], v[104:107], v[32:47]\n\t" \
  "ds_read_b64_tr_b16 v[92:93], v135 offset:20480\n\t" \
  "ds_read_b64_tr_b16 v[94:95], v135 offset:21504\n\t" \
  "v_mfma_f32_32x32x16_bf16 v[48:63], v[120:123], v[104:107], v[48:63]\n\t" \
  "ds_read_b64_tr_b16 v[96:97], v136 offset:20480\n\t" \
  "ds_read_b64_tr_b16 v[98:99], v136 offset:21504\n\t" \
  "s_waitcnt lgkmcnt(9)\n\t" \
  "v_mfma_f32_32x32x16_bf16 v[64:79], v[124:127], v[104:107], v[64:79]\n\t" \
  "ds_read_b64_tr_b16 v[100:101], v137 offset:20480\n\t" \
  "ds_read_b64_tr_b16 v[102:103], v137 offset:20992\n\t" \
  "s_waitcnt lgkmcnt(6)\n\t" \
  "v_mfma_f32_32x32x16_bf16 v[0:15], v[84:87], v[80:83], v[0:15]\n\t" \
  "ds_read_b128 v[104:107], v141 offset:16384\n\t" \
  "ds_read_b64_tr_b16 v[108:109], v133 offset:24576\n\t" \
  "ds_read_b64_tr_b16 v[110:111], v133 offset:25600\n\t" \
  "v_mfma_f32_32x32x16_bf16 v[16:31], v[88:91], v[80:83], v[16:31]\n\t" \
  "ds_read_b64_tr_b16 v[112:113], v134 offset:24576\n\t" \
  "ds_read_b64_tr_b16 v[114:115], v134 offset:25600\n\t" \
  "s_waitcnt lgkmcnt(7)\n\t" \
  "v_mfma_f32_32x32x16_bf16 v[32:47], v[92:95], v[80:83], v[32:47]\n\t" \
  "ds_read_b64_tr_b16 v[116:117], v135 offset:24576\n\t" \
  "ds_read_b64_tr_b16 v[118:119], v135 offset:25600\n\t" \
  "v_mfma_f32_32x32x16_bf16 v[48:63], v[96:99], v[80:83], v[48:63]\n\t" \
  "ds_read_b64_tr_b16 v[120:121], v136 offset:24576\n\t" \
  "ds_read_b64_tr_b16 v[122:123], v136 offset:25600\n\t" \
  "s_waitcnt lgkmcnt(9)\n\t" \
  "v_mfma_f32_32x32x16_bf16 v[64:79], v[100:103], v[80:83], v[64:79]\n\t" \
  "ds_read_b64_tr_b16 v[124:125], v137 offset:22528\n\t" \
  "ds_read_b64_tr_b16 v[126:127], v137 offset:23040\n\t" \
  "s_waitcnt lgkmcnt(6)\n\t" \
  "v_mfma_f32_32x32x16_bf16 v[0:15], v[108:111], v[104:107], v[0:15]\n\t" \
  "s_barrier\n\t" \
  "ds_read_b128 v[80:83], v138 offset:32768\n\t" \
  "ds_read_b64_tr_b16 v[84:85], v128 offset:0\n\t" \
  "ds_read_b64_tr_b16 v[86:87], v128 offset:1024\n\t" \
  "v_mfma_f32_32x32x16_bf16 v[16:31], v[112:115], v[104:107], v[16:31]\n\t" \
  "ds_read_b64_tr_b16 v[88:89], v129 offset:0\n\t" \
  "ds_read_b64_tr_b16 v[90:91], v129 offset:1024\n\t" \
  "s_waitcnt lgkmcnt(7)\n\t" \
  "v_mfma_f32_32x32x16_bf16 v[32:47], v[116:119], v[104:107], v[32:47]\n\t" \
  "ds_read_b64_tr_b16 v[92:93], v130 offset:0\n\t" \
  "ds_read_b64_tr_b16 v[94:95], v130 offset:1024\n\t" \
  "v_mfma_f32_32x32x16_bf16 v[48:63], v[120:123], v[104:107], v[48:63]\n\t" \
  "ds_read_b64_tr_b16 v[96:97], v131 offset:0\n\t" \
  "ds_read_b64_tr_b16 v[98:99], v131 offset:1024\n\t" \
  "s_waitcnt lgkmcnt(9)\n\t" \
  "v_mfma_f32_32x32x16_bf16 v[64:79], v[124:127], v[104:107], v[64:79]\n\t" \
  "ds_read_b64_tr_b16 v[100:101], v132 offset:0\n\t" \
  "ds_read_b64_tr_b16 v[102:103], v132 offset:512\n\t" \
  "s_waitcnt lgkmcnt(6)\n\t" \
  "v_mfma_f32_32x32x16_bf16 v[0:15], v[84:87], v[80:83], v[0:15]\n\t" \
  "ds_read_b128 v[104:107], v139 offset:32768\n\t" \
  "ds_read_b64_tr_b16 v[108:109], v128 offset:4096\n\t" \
  "ds_read_b64_tr_b16 v[110:111], v128 offset:5120\n\t" \
  "v_mfma_f32_32x32x16_bf16 v[16:31], v[88:91], v[80:83], v[16:31]\n\t" \
  "ds_read_b64_tr_b16 v[112:113], v129 offset:4096\n\t" \
  "ds_read_b64_tr_b16 v[114:115], v129 offset:5120\n\t" \
  "s_waitcnt lgkmcnt(7)\n\t" \
  "v_mfma_f32_32x32x16_bf16 v[32:47], v[92:95], v[80:83], v[32:47]\n\t" \
  "ds_read_b64_tr_b16 v[116:117], v130 offset:4096\n\t" \
  "ds_read_b64_tr_b16 v[118:119], v130 offset:5120\n\t" \
  "v_mfma_f32_32x32x16_bf16 v[48:63], v[96:99], v[80:83], v[48:63]\n\t" \
  "ds_read_b64_tr_b16 v[120:121], v131 offset:4096\n\t" \
  "ds_read_b64_tr_b16 v[122:123], v131 offset:5120\n\t" \
  "s_waitcnt lgkmcnt(9)\n\t" \
  "v_mfma_f32_32x32x16_bf16 v[64:79], v[100:103], v[80:83], v[64:79]\n\t" \
  "ds_read_b64_tr_b16 v[124:125], v132 offset:2048\n\t" \
  "ds_read_b64_tr_b16 v[126:127], v132 offset:2560\n\t" \
  "s_waitcnt lgkmcnt(6)\n\t" \
  "v_mfma_f32_32x32x16_bf16 v[0:15], v[108:111], v[104:107], v[0:15]\n\t" \
  "s_barrier\n\t" \
  "ds_read_b128 v[80:83], v140 offset:32768\n\t" \
  "ds_read_b64_tr_b16 v[84:85], v128 offset:20480\n\t" \
  "ds_read_b64_tr_b16 v[86:87], v128 offset:21504\n\t" \
  "v_mfma_f32_32x32x16_bf16 v[16:31], v[112:115], v[104:107], v[16:31]\n\t" \
  "ds_read_b64_tr_b16 v[88:89], v129 offset:20480\n\t" \
  "ds_read_b64_tr_b16 v[90:91], v129 offset:21504\n\t" \
  "s_waitcnt lgkmcnt(7)\n\t" \
  "v_mfma_f32_32x32x16_bf16 v[32:47], v[116:119], v[104:107], v[32:47]\n\t" \
  "ds_read_b64_tr_b16 v[92:93], v130 offset:20480\n\t" \
  "ds_read_b64_tr_b16 v[94:95], v130 offset:21504\n\t" \
  "v_mfma_f32_32x32x16_bf16 v[48:63], v[120:123], v[104:107], v[48:63]\n\t" \
  "ds_read_b64_tr_b16 v[96:97], v131 offset:20480\n\t" \
  "ds_read_b64_tr_b16 v[98:99], v131 offset:21504\n\t" \
  "s_waitcnt lgkmcnt(9)\n\t" \
  "v_mfma_f32_32x32x16_bf16 v[64:79], v[124:127], v[104:107], v[64:79]\n\t" \
  "ds_read_b64_tr_b16 v[100:101], v132 offset:20480\n\t" \
  "ds_read_b64_tr_b16 v[102:103], v132 offset:20992\n\t" \
  "s_waitcnt lgkmcnt(6)\n\t" \
  "v_mfma_f32_32x32x16_bf16 v[0:15], v[84:87], v[80:83], v[0:15]\n\t" \
  "ds_read_b128 v[104:107], v141 offset:32768\n\t" \
  "ds_read_b64_tr_b16 v[108:109], v128 offset:24576\n\t" \
  "ds_read_b64_tr_b16 v[110:111], v128 offset:25600\n\t" \
  "v_mfma_f32_32x32x16_bf16 v[16:31], v[88:91], v[80:83], v[16:31]\n\t" \
  "ds_read_b64_tr_b16 v[112:113], v129 offset:24576\n\t" \
  "ds_read_b64_tr_b16 v[114:115], v129 offset:25600\n\t" \
  "s_waitcnt lgkmcnt(7)\n\t" \
  "v_mfma_f32_32x32x16_bf16 v[32:47], v[92:95], v[80:83], v[32:47]\n\t" \
  "ds_read_b64_tr_b16 v[116:117], v130 offset:24576\n\t" \
  "ds_read_b64_tr_b16 v[118:119], v130 offset:25600\n\t" \
  "v_mfma_f32_32x32x16_bf16 v[48:63], v[96:99], v[80:83], v[48:63]\n\t" \
  "ds_read_b64_tr_b16 v[120:121], v131 offset:24576\n\t" \
  "ds_read_b64_tr_b16 v[122:123], v131 offset:25600\n\t" \
  "s_waitcnt lgkmcnt(9)\n\t" \
  "v_mfma_f32_32x32x16_bf16 v[64:79], v[100:103], v[80:83], v[64:79]\n\t" \
  "ds_read_b64_tr_b16 v[124:125], v132 offset:22528\n\t" \
  "ds_read_b64_tr_b16 v[126:127], v132 offset:23040\n\t" \
  "s_waitcnt lgkmcnt(6)\n\t" \
  "v_mfma_f32_32x32x16_bf16 v[0:15], v[108:111], v[104:107], v[0:15]\n\t" \
  "s_barrier\n\t" \
  "ds_read_b128 v[80:83], v138 offset:0\n\t" \
  "ds_read_b64_tr_b16 v[84:85], v128 offset:40960\n\t" \
  "ds_read_b64_tr_b16 v[86:87], v128 offset:41984\n\t" \
  "v_mfma_f32_32x32x16_bf16 v[16:31], v[112:115], v[104:107], v[16:31]\n\t" \
  "ds_read_b64_tr_b16 v[88:89], v129 offset:40960\n\t" \
  "ds_read_b64_tr_b16 v[90:91], v129 offset:41984\n\t" \
  "s_waitcnt lgkmcnt(7)\n\t" \
  "v_mfma_f32_32x32x16_bf16 v[32:47], v[116:119], v[104:107], v[32:47]\n\t" \
  "ds_read_b64_tr_b16 v[92:93], v130 offset:40960\n\t" \
  "ds_read_b64_tr_b16 v[94:95], v130 offset:41984\n\t" \
  "v_mfma_f32_32x32x16_bf16 v[48:63], v[120:123], v[104:107], v[48:63]\n\t" \
  "ds_read_b64_tr_b16 v[96:97], v131 offset:40960\n\t" \
  "ds_read_b64_tr_b16 v[98:99], v131 offset:41984\n\t" \
  "s_waitcnt lgkmcnt(9)\n\t" \
  "v_mfma_f32_32x32x16_bf16 v[64:79], v[124:127], v[104:107], v[64:79]\n\t" \
  "ds_read_b64_tr_b16 v[100:101], v132 offset:40960\n\t" \
  "ds_read_b64_tr_b16 v[102:103], v132 offset:41472\n\t" \
  "s_waitcnt lgkmcnt(6)\n\t" \
  "v_mfma_f32_32x32x16_bf16 v[0:15], v[84:87], v[80:83], v[0:15]\n\t" \
  "ds_read_b128 v[104:107], v139 offset:0\n\t" \
  "ds_read_b64_tr_b16 v[108:109], v128 offset:45056\n\t" \
  "ds_read_b64_tr_b16 v[110:111], v128 offset:46080\n\t" \
  "v_mfma_f32_32x32x16_bf16 v[16:31], v[88:91], v[80:83], v[16:31]\n\t" \
  "ds_read_b64_tr_b16 v[112:113], v129 offset:45056\n\t" \
  "ds_read_b64_tr_b16 v[114:115], v129 offset:46080\n\t" \
  "s_waitcnt lgkmcnt(7)\n\t" \
  "v_mfma_f32_32x32x16_bf16 v[32:47], v[92:95], v[80:83], v[32:47]\n\t" \
  "ds_read_b64_tr_b16 v[116:117], v130 offset:45056\n\t" \
  "ds_read_b64_tr_b16 v[118:119], v130 offset:46080\n\t" \
  "v_mfma_f32_32x32x16_bf16 v[48:63], v[96:99], v[80:83], v[48:63]\n\t" \
  "ds_read_b64_tr_b16 v[120:121], v131 offset:45056\n\t" \
  "ds_read_b64_tr_b16 v[122:123], v131 offset:46080\n\t" \
  "s_waitcnt lgkmcnt(9)\n\t" \
  "v_mfma_f32_32x32x16_bf16 v[64:79], v[100:103], v[80:83], v[64:79]\n\t" \
  "ds_read_b64_tr_b16 v[124:125], v132 offset:43008\n\t" \
  "ds_read_b64_tr_b16 v[126:127], v132 offset:43520\n\t" \
  "s_waitcnt lgkmcnt(6)\n\t" \
  "v_mfma_f32_32x32x16_bf16 v[0:15], v[108:111], v[104:107], v[0:15]\n\t" \
  "s_barrier\n\t" \
  "ds_read_b128 v[80:83], v140 offset:0\n\t" \
  "ds_read_b64_tr_b16 v[84:85], v133 offset:0\n\t" \
  "ds_read_b64_tr_b16 v[86:87], v133 offset:1024\n\t" \
  "v_mfma_f32_32x32x16_bf16 v[16:31], v[112:115], v[104:107], v[16:31]\n\t" \
  "ds_read_b64_tr_b16 v[88:89], v134 offset:0\n\t" \
  "ds_read_b64_tr_b16 v[90:91], v134 offset:1024\n\t" \
  "s_waitcnt lgkmcnt(7)\n\t" \
  "v_mfma_f32_32x32x16_bf16 v[32:47], v[116:119], v[104:107], v[32:47]\n\t" \
  "ds_read_b64_tr_b16 v[92:93], v135 offset:0\n\t" \
  "ds_read_b64_tr_b16 v[94:95], v135 offset:1024\n\t" \
  "v_mfma_f32_32x32x16_bf16 v[48:63], v[120:123], v[104:107], v[48:63]\n\t" \
  "ds_read_b64_tr_b16 v[96:97], v136 offset:0\n\t" \
  "ds_read_b64_tr_b16 v[98:99], v136 offset:1024\n\t" \
  "s_waitcnt lgkmcnt(9)\n\t" \
  "v_mfma_f32_32x32x16_bf16 v[64:79], v[124:127], v[104:107], v[64:79]\n\t" \
  "ds_read_b64_tr_b16 v[100:101], v137 offset:0\n\t" \
  "ds_read_b64_tr_b16 v[102:103], v137 offset:512\n\t" \
  "s_waitcnt lgkmcnt(6)\n\t" \
  "v_mfma_f32_32x32x16_bf16 v[0:15], v[84:87], v[80:83], v[0:15]\n\t" \
  "ds_read_b128 v[104:107], v141 offset:0\n\t" \
  "ds_read_b64_tr_b16 v[108:109], v133 offset:4096\n\t" \
  "ds_read_b64_tr_b16 v[110:111], v133 offset:5120\n\t" \
  "v_mfma_f32_32x32x16_bf16 v[16:31], v[88:91], v[80:83], v[16:31]\n\t" \
  "ds_read_b64_tr_b16 v[112:113], v134 offset:4096\n\t" \
  "ds_read_b64_tr_b16 v[114:115], v134 offset:5120\n\t" \
  "s_waitcnt lgkmcnt(7)\n\t" \
  "v_mfma_f32_32x32x16_bf16 v[32:47], v[92:95], v[80:83], v[32:47]\n\t" \
  "ds_read_b64_tr_b16 v[116:117], v135 offset:4096\n\t" \
  "ds_read_b64_tr_b16 v[118:119], v135 offset:5120\n\t" \
  "v_mfma_f32_32x32x16_bf16 v[48:63], v[96:99], v[80:83], v[48:63]\n\t" \
  "ds_read_b64_tr_b16 v[120:121], v136 offset:4096\n\t" \
  "ds_read_b64_tr_b16 v[122:123], v136 offset:5120\n\t" \
  "s_waitcnt lgkmcnt(9)\n\t" \
  "v_mfma_f32_32x32x16_bf16 v[64:79], v[100:103], v[80:83], v[64:79]\n\t" \
  "ds_read_b64_tr_b16 v[124:125], v137 offset:2048\n\t" \
  "ds_read_b64_tr_b16 v[126:127], v137 offset:2560\n\t" \
  "s_waitcnt lgkmcnt(6)\n\t" \
  "v_mfma_f32_32x32x16_bf16 v[0:15], v[108:111], v[104:107], v[0:15]\n\t" \
  "s_barrier\n\t" \
  "ds_read_b128 v[80:83], v138 offset:16384\n\t" \
  "ds_read_b64_tr_b16 v[84:85], v133 offset:20480\n\t" \
  "ds_read_b64_tr_b16 v[86:87], v133 offset:21504\n\t" \
  "v_mfma_f32_32x32x16_bf16 v[16:31], v[112:115], v[104:107], v[16:31]\n\t" \
  "ds_read_b64_tr_b16 v[88:89], v134 offset:20480\n\t" \
  "ds_read_b64_tr_b16 v[90:91], v134 offset:21504\n\t" \
  "s_waitcnt lgkmcnt(7)\n\t" \
  "v_mfma_f32_32x32x16_bf16 v[32:47], v[116:119], v[104:107], v[32:47]\n\t" \
  "ds_read_b64_tr_b16 v[92:93], v135 offset:20480\n\t" \
  "ds_read_b64_tr_b16 v[94:95], v135 offset:21504\n\t" \
  "v_mfma_f32_32x32x16_bf16 v[48:63], v[120:123], v[104:107], v[48:63]\n\t" \
  "ds_read_b64_tr_b16 v[96:97], v136 offset:20480\n\t" \
  "ds_read_b64_tr_b16 v[98:99], v136 offset:21504\n\t" \
  "s_waitcnt lgkmcnt(9)\n\t" \
  "v_mfma_f32_32x32x16_bf16 v[64:79], v[124:127], v[104:107], v[64:79]\n\t" \
  "ds_read_b64_tr_b16 v[100:101], v137 offset:20480\n\t" \
  "ds_read_b64_tr_b16 v[102:103], v137 offset:20992\n\t" \
  "s_waitcnt lgkmcnt(6)\n\t" \
  "v_mfma_f32_32x32x16_bf16 v[0:15], v[84:87], v[80:83], v[0:15]\n\t" \
  "ds_read_b128 v[104:107], v139 offset:16384\n\t" \
  "ds_read_b64_tr_b16 v[108:109], v133 offset:24576\n\t" \
  "ds_read_b64_tr_b16 v[110:111], v133 offset:25600\n\t" \
  "v_mfma_f32_32x32x16_bf16 v[16:31], v[88:91], v[80:83], v[16:31]\n\t" \
  "ds_read_b64_tr_b16 v[112:113], v134 offset:24576\n\t" \
  "ds_read_b64_tr_b16 v[114:115], v134 offset:25600\n\t" \
  "s_waitcnt lgkmcnt(7)\n\t" \
  "v_mfma_f32_32x32x16_bf16 v[32:47], v[92:95], v[80:83], v[32:47]\n\t" \
  "ds_read_b64_tr_b16 v[116:117], v135 offset:24576\n\t" \
  "ds_read_b64_tr_b16 v[118:119], v135 offset:25600\n\t" \
  "v_mfma_f32_32x32x16_bf16 v[48:63], v[96:99], v[80:83], v[48:63]\n\t" \
  "ds_read_b64_tr_b16 v[120:121], v136 offset:24576\n\t" \
  "ds_read_b64_tr_b16 v[122:123], v136 offset:25600\n\t" \
  "s_waitcnt lgkmcnt(9)\n\t" \
  "v_mfma_f32_32x32x16_bf16 v[64:79], v[100:103], v[80:83], v[64:79]\n\t" \
  "ds_read_b64_tr_b16 v[124:125], v137 offset:22528\n\t" \
  "ds_read_b64_tr_b16 v[126:127], v137 offset:23040\n\t" \
  "s_waitcnt lgkmcnt(6)\n\t" \
  "v_mfma_f32_32x32x16_bf16 v[0:15], v[108:111], v[104:107], v[0:15]\n\t" \
  "s_barrier\n\t" \
  "ds_read_b128 v[80:83], v140 offset:16384\n\t" \
  "ds_read_b64_tr_b16 v[84:85], v128 offset:0\n\t" \
  "ds_read_b64_tr_b16 v[86:87], v128 offset:1024\n\t" \
  "v_mfma_f32_32x32x16_bf16 v[16:31], v[112:115], v[104:107], v[16:31]\n\t" \
  "ds_read_b64_tr_b16 v[88:89], v129 offset:0\n\t" \
  "ds_read_b64_tr_b16 v[90:91], v129 offset:1024\n\t" \
  "s_waitcnt lgkmcnt(7)\n\t" \
  "v_mfma_f32_32x32x16_bf16 v[32:47], v[116:119], v[104:107], v[32:47]\n\t" \
  "ds_read_b64_tr_b16 v[92:93], v130 offset:0\n\t" \
  "ds_read_b64_tr_b16 v[94:95], v130 offset:1024\n\t" \
  "v_mfma_f32_32x32x16_bf16 v[48:63], v[120:123], v[104:107], v[48:63]\n\t" \
  "ds_read_b64_tr_b16 v[96:97], v131 offset:0\n\t" \
  "ds_read_b64_tr_b16 v[98:99], v131 offset:1024\n\t" \
  "s_waitcnt lgkmcnt(9)\n\t" \
  "v_mfma_f32_32x32x16_bf16 v[64:79], v[124:127], v[104:107], v[64:79]\n\t" \
  "ds_read_b64_tr_b16 v[100:101], v132 offset:0\n\t" \
  "ds_read_b64_tr_b16 v[102:103], v132 offset:512\n\t" \
  "s_waitcnt lgkmcnt(6)\n\t" \
  "v_mfma_f32_32x32x16_bf16 v[0:15], v[84:87], v[80:83], v[0:15]\n\t" \
  "ds_read_b128 v[104:107], v141 offset:16384\n\t" \
  "ds_read_b64_tr_b16 v[108:109], v128 offset:4096\n\t" \
  "ds_read_b64_tr_b16 v[110:111], v128 offset:5120\n\t" \
  "v_mfma_f32_32x32x16_bf16 v[16:31], v[88:91], v[80:83], v[16:31]\n\t" \
  "ds_read_b64_tr_b16 v[112:113], v129 offset:4096\n\t" \
  "ds_read_b64_tr_b16 v[114:115], v129 offset:5120\n\t" \
  "s_waitcnt lgkmcnt(7)\n\t" \
  "v_mfma_f32_32x32x16_bf16 v[32:47], v[92:95], v[80:83], v[32:47]\n\t" \
  "ds_read_b64_tr_b16 v[116:117], v130 offset:4096\n\t" \
  "ds_read_b64_tr_b16 v[118:119], v130 offset:5120\n\t" \
  "v_mfma_f32_32x32x16_bf16 v[48:63], v[96:99], v[80:83], v[48:63]\n\t" \
  "ds_read_b64_tr_b16 v[120:121], v131 offset:4096\n\t" \
  "ds_read_b64_tr_b16 v[122:123], v131 offset:5120\n\t" \
  "s_waitcnt lgkmcnt(9)\n\t" \
  "v_mfma_f32_32x32x16_bf16 v[64:79], v[100:103], v[80:83], v[64:79]\n\t" \
  "ds_read_b64_tr_b16 v[124:125], v132 offset:2048\n\t" \
  "ds_read_b64_tr_b16 v[126:127], v132 offset:2560\n\t" \
  "s_waitcnt lgkmcnt(6)\n\t" \
  "v_mfma_f32_32x32x16_bf16 v[0:15], v[108:111], v[104:107], v[0:15]\n\t" \
  "v_mfma_f32_32x32x16_bf16 v[16:31], v[112:115], v[104:107], v[16:31]\n\t" \
  "s_waitcnt lgkmcnt(2)\n\t" \
  "v_mfma_f32_32x32x16_bf16 v[32:47], v[116:119], v[104:107], v[32:47]\n\t" \
  "v_mfma_f32_32x32x16_bf16 v[48:63], v[120:123], v[104:107], v[48:63]\n\t" \
  "s_waitcnt lgkmcnt(0)\n\t" \
  "v_mfma_f32_32x32x16_bf16 v[64:79], v[124:127], v[104:107], v[64:79]\n\t" \
  "s_nop 15\n\t"
#define G1_LOOP_Y \
  "s_barrier\n\t" \
  "ds_read_b128 v[80:83], v138 offset:0\n\t" \
  "ds_read_b64_tr_b16 v[84:85], v128 offset:0\n\t" \
  "ds_read_b64_tr_b16 v[86:87], v128 offset:2048\n\t" \
  "ds_read_b64_tr_b16 v[88:89], v129 offset:0\n\t" \
  "ds_read_b64_tr_b16 v[90:91], v129 offset:2048\n\t" \
  "ds_read_b64_tr_b16 v[92:93], v130 offset:0\n\t" \
  "ds_read_b64_tr_b16 v[94:95], v130 offset:2048\n\t" \
  "ds_read_b64_tr_b16 v[96:97], v131 offset:0\n\t" \
  "ds_read_b64_tr_b16 v[98:99], v131 offset:2048\n\t" \
  "ds_read_b64_tr_b16 v[100:101], v132 offset:0\n\t" \
  "ds_read_b64_tr_b16 v[102:103], v132 offset:512\n\t" \
  "s_waitcnt lgkmcnt(6)\n\t" \
  "v_mfma_f32_32x32x16_bf16 v[0:15], v[80:83], v[84:87], v[0:15]\n\t" \
  "ds_read_b128 v[104:107], v139 offset:0\n\t" \
  "ds_read_b64_tr_b16 v[108:109], v128 offset:8192\n\t" \
  "ds_read_b64_tr_b16 v[110:111], v128 offset:10240\n\t" \
  "v_mfma_f32_32x32x16_bf16 v[16:31], v[80:83], v[88:91], v[16:31]\n\t" \
  "ds_read_b64_tr_b16 v[112:113], v129 offset:8192\n\t" \
  "ds_read_b64_tr_b16 v[114:115], v129 offset:10240\n\t" \
  "s_waitcnt lgkmcnt(7)\n\t" \
  "v_mfma_f32_32x32x16_bf16 v[32:47], v[80:83], v[92:95], v[32:47]\n\t" \
  "ds_read_b64_tr_b16 v[116:117], v130 offset:8192\n\t" \
  "ds_read_b64_tr_b16 v[118:119], v130 offset:10240\n\t" \
  "v_mfma_f32_32x32x16_bf16 v[48:63], v[80:83], v[96:99], v[48:63]\n\t" \
  "ds_read_b64_tr_b16 v[120:121], v131 offset:8192\n\t" \
  "ds_read_b64_tr_b16 v[122:123], v131 offset:10240\n\t" \
  "s_waitcnt lgkmcnt(9)\n\t" \
  "v_mfma_f32_32x32x16_bf16 v[64:79], v[100:103], v[80:83], v[64:79]\n\t" \
  "ds_read_b64_tr_b16 v[124:125], v132 offset:2048\n\t" \
  "ds_read_b64_tr_b16 v[126:127], v132 offset:2560\n\t" \
  "s_waitcnt lgkmcnt(6)\n\t" \
  "v_mfma_f32_32x32x16_bf16 v[0:15], v[104:107], v[108:111], v[0:15]\n\t" \
  "s_barrier\n\t" \
  "ds_read_b128 v[80:83], v140 offset:0\n\t" \
  "ds_read_b64_tr_b16 v[84:85], v128 offset:20480\n\t" \
  "ds_read_b64_tr_b16 v[86:87], v128 offset:22528\n\t" \
  "v_mfma_f32_32x32x16_bf16 v[16:31], v[104:107], v[112:115], v[16:31]\n\t" \
  "ds_read_b64_tr_b16 v[88:89], v129 offset:20480\n\t" \
  "ds_read_b64_tr_b16 v[90:91], v129 offset:22528\n\t" \
  "s_waitcnt lgkmcnt(7)\n\t" \
  "v_mfma_f32_32x32x16_bf16 v[32:47], v[104:107], v[116:119], v[32:47]\n\t" \
  "ds_read_b64_tr_b16 v[92:93], v130 offset:20480\n\t" \
  "ds_read_b64_tr_b16 v[94:95], v130 offset:22528\n\t" \
  "v_mfma_f32_32x32x16_bf16 v[48:63], v[104:107], v[120:123], v[48:63]\n\t" \
  "ds_read_b64_tr_b16 v[96:97], v131 offset:20480\n\t" \
  "ds_read_b64_tr_b16 v[98:99], v131 offset:22528\n\t" \
  "s_waitcnt lgkmcnt(9)\n\t" \
  "v_mfma_f32_32x32x16_bf16 v[64:79], v[124:127], v[104:107], v[64:79]\n\t" \
  "ds_read_b64_tr_b16 v[100:101], v132 offset:20480\n\t" \
  "ds_read_b64_tr_b16 v[102:103], v132 offset:20992\n\t" \
  "s_waitcnt lgkmcnt(6)\n\t" \
  "v_mfma_f32_32x32x16_bf16 v[0:15], v[80:83], v[84:87], v[0:15]\n\t" \
  "ds_read_b128 v[104:107], v141 offset:0\n\t" \
  "ds_read_b64_tr_b16 v[108:109], v128 offset:28672\n\t" \
  "ds_read_b64_tr_b16 v[110:111], v128 offset:30720\n\t" \
  "v_mfma_f32_32x32x16_bf16 v[16:31], v[80:83], v[88:91], v[16:31]\n\t" \
  "ds_read_b64_tr_b16 v[112:113], v129 offset:28672\n\t" \
  "ds_read_b64_tr_b16 v[114:115], v129 offset:30720\n\t" \
  "s_waitcnt lgkmcnt(7)\n\t" \
  "v_mfma_f32_32x32x16_bf16 v[32:47], v[80:83], v[92:95], v[32:47]\n\t" \
  "ds_read_b64_tr_b16 v[116:117], v130 offset:28672\n\t" \
  "ds_read_b64_tr_b16 v[118:119], v130 offset:30720\n\t" \
  "v_mfma_f32_32x32x16_bf16 v[48:63], v[80:83], v[96:99], v[48:63]\n\t" \
  "ds_read_b64_tr_b16 v[120:121], v131 offset:28672\n\t" \
  "ds_read_b64_tr_b16 v[122:123], v131 offset:30720\n\t" \
  "s_waitcnt lgkmcnt(9)\n\t" \
  "v_mfma_f32_32x32x16_bf16 v[64:79], v[100:103], v[80:83], v[64:79]\n\t" \
  "ds_read_b64_tr_b16 v[124:125], v132 offset:22528\n\t" \
  "ds_read_b64_tr_b16 v[126:127], v132 offset:23040\n\t" \
  "s_waitcnt lgkmcnt(6)\n\t" \
  "v_mfma_f32_32x32x16_bf16 v[0:15], v[104:107], v[108:111], v[0:15]\n\t" \
  "s_barrier\n\t" \
  "ds_read_b128 v[80:83], v138 offset:16384\n\t" \
  "ds_read_b64_tr_b16 v[84:85], v128 offset:40960\n\t" \
  "ds_read_b64_tr_b16 v[86:87], v128 offset:43008\n\t" \
  "v_mfma_f32_32x32x16_bf16 v[16:31], v[104:107], v[112:115], v[16:31]\n\t" \
  "ds_read_b64_tr_b16 v[88:89], v129 offset:40960\n\t" \
  "ds_read_b64_tr_b16 v[90:91], v129 offset:43008\n\t" \
  "s_waitcnt lgkmcnt(7)\n\t" \
  "v_mfma_f32_32x32x16_bf16 v[32:47], v[104:107], v[116:119], v[32:47]\n\t" \
  "ds_read_b64_tr_b16 v[92:93], v130 offset:40960\n\t" \
  "ds_read_b64_tr_b16 v[94:95], v130 offset:43008\n\t" \
  "v_mfma_f32_32x32x16_bf16 v[48:63], v[104:107], v[120:123], v[48:63]\n\t" \
  "ds_read_b64_tr_b16 v[96:97], v131 offset:40960\n\t" \
  "ds_read_b64_tr_b16 v[98:99], v131 offset:43008\n\t" \
  "s_waitcnt lgkmcnt(9)\n\t" \
  "v_mfma_f32_32x32x16_bf16 v[64:79], v[124:127], v[104:107], v[64:79]\n\t" \
  "ds_read_b64_tr_b16 v[100:101], v132 offset:40960\n\t" \
  "ds_read_b64_tr_b16 v[102:103], v132 offset:41472\n\t" \
  "s_waitcnt lgkmcnt(6)\n\t" \
  "v_mfma_f32_32x32x16_bf16 v[0:15], v[80:83], v[84:87], v[0:15]\n\t" \
  "ds_read_b128 v[104:107], v139 offset:16384\n\t" \
  "ds_read_b64_tr_b16 v[108:109], v128 offset:49152\n\t" \
  "ds_read_b64_tr_b16 v[110:111], v128 offset:51200\n\t" \
  "v_mfma_f32_32x32x16_bf16 v[16:31], v[80:83], v[88:91], v[16:31]\n\t" \
  "ds_read_b64_tr_b16 v[112:113], v129 offset:49152\n\t" \
  "ds_read_b64_tr_b16 v[114:115], v129 offset:51200\n\t" \
  "s_waitcnt lgkmcnt(7)\n\t" \
  "v_mfma_f32_32x32x16_bf16 v[32:47], v[80:83], v[92:95], v[32:47]\n\t" \
  "ds_read_b64_tr_b16 v[116:117], v130 offset:49152\n\t" \
  "ds_read_b64_tr_b16 v[118:119], v130 offset:51200\n\t" \
  "v_mfma_f32_32x32x16_bf16 v[48:63], v[80:83], v[96:99], v[48:63]\n\t" \
  "ds_read_b64_tr_b16 v[120:121], v131 offset:49152\n\t" \
  "ds_read_b64_tr_b16 v[122:123], v131 offset:51200\n\t" \
  "s_waitcnt lgkmcnt(9)\n\t" \
  "v_mfma_f32_32x32x16_bf16 v[64:79], v[100:103], v[80:83], v[64:79]\n\t" \
  "ds_read_b64_tr_b16 v[124:125], v132 offset:43008\n\t" \
  "ds_read_b64_tr_b16 v[126:127], v132 offset:43520\n\t" \
  "s_waitcnt lgkmcnt(6)\n\t" \
  "v_mfma_f32_32x32x16_bf16 v[0:15], v[104:107], v[108:111], v[0:15]\n\t" \
  "s_barrier\n\t" \
  "ds_read_b128 v[80:83], v140 offset:16384\n\t" \
  "ds_read_b64_tr_b16 v[84:85], v133 offset:0\n\t" \
  "ds_read_b64_tr_b16 v[86:87], v133 offset:2048\n\t" \
  "v_mfma_f32_32x32x16_bf16 v[16:31], v[104:107], v[112:115], v[16:31]\n\t" \
  "ds_read_b64_tr_b16 v[88:89], v134 offset:0\n\t" \
  "ds_read_b64_tr_b16 v[90:91], v134 offset:2048\n\t" \
  "s_waitcnt lgkmcnt(7)\n\t" \
  "v_mfma_f32_32x32x16_bf16 v[32:47], v[104:107], v[116:119], v[32:47]\n\t" \
  "ds_read_b64_tr_b16 v[92:93], v135 offset:0\n\t" \
  "ds_read_b64_tr_b16 v[94:95], v135 offset:2048\n\t" \
  "v_mfma_f32_32x32x16_bf16 v[48:63], v[104:107], v[120:123], v[48:63]\n\t" \
  "ds_read_b64_tr_b16 v[96:97], v136 offset:0\n\t" \
  "ds_read_b64_tr_b16 v[98:99], v136 offset:2048\n\t" \
  "s_waitcnt lgkmcnt(9)\n\t" \
  "v_mfma_f32_32x32x16_bf16 v[64:79], v[124:127], v[104:107], v[64:79]\n\t" \
  "ds_read_b64_tr_b16 v[100:101], v137 offset:0\n\t" \
  "ds_read_b64_tr_b16 v[102:103], v137 offset:512\n\t" \
  "s_waitcnt lgkmcnt(6)\n\t" \
  "v_mfma_f32_32x32x16_bf16 v[0:15], v[80:83], v[84:87], v[0:15]\n\t" \
  "ds_read_b128 v[104:107], v141 offset:16384\n\t" \
  "ds_read_b64_tr_b16 v[108:109], v133 offset:8192\n\t" \
  "ds_read_b64_tr_b16 v[110:111], v133 offset:10240\n\t" \
  "v_mfma_f32_32x32x16_bf16 v[16:31], v[80:83], v[88:91], v[16:31]\n\t" \
  "ds_read_b64_tr_b16 v[112:113], v134 offset:8192\n\t" \
  "ds_read_b64_tr_b16 v[114:115], v134 offset:10240\n\t" \
  "s_waitcnt lgkmcnt(7)\n\t" \
  "v_mfma_f32_32x32x16_bf16 v[32:47], v[80:83], v[92:95], v[32:47]\n\t" \
  "ds_read_b64_tr_b16 v[116:117], v135 offset:8192\n\t" \
  "ds_read_b64_tr_b16 v[118:119], v135 offset:10240\n\t" \
  "v_mfma_f32_32x32x16_bf16 v[48:63], v[80:83], v[96:99], v[48:63]\n\t" \
  "ds_read_b64_tr_b16 v[120:121], v136 offset:8192\n\t" \
  "ds_read_b64_tr_b16 v[122:123], v136 offset:10240\n\t" \
  "s_waitcnt lgkmcnt(9)\n\t" \
  "v_mfma_f32_32x32x16_bf16 v[64:79], v[100:103], v[80:83], v[64:79]\n\t" \
  "ds_read_b64_tr_b16 v[124:125], v137 offset:2048\n\t" \
  "ds_read_b64_tr_b16 v[126:127], v137 offset:2560\n\t" \
  "s_waitcnt lgkmcnt(6)\n\t" \
  "v_mfma_f32_32x32x16_bf16 v[0:15], v[104:107], v[108:111], v[0:15]\n\t" \
  "s_barrier\n\t" \
  "ds_read_b128 v[80:83], v138 offset:32768\n\t" \
  "ds_read_b64_tr_b16 v[84:85], v133 offset:20480\n\t" \
  "ds_read_b64_tr_b16 v[86:87], v133 offset:22528\n\t" \
  "v_mfma_f32_32x32x16_bf16 v[16:31], v[104:107], v[112:115], v[16:31]\n\t" \
  "ds_read_b64_tr_b16 v[88:89], v134 offset:20480\n\t" \
  "ds_read_b64_tr_b16 v[90:91], v134 offset:22528\n\t" \
  "s_waitcnt lgkmcnt(7)\n\t" \
  "v_mfma_f32_32x32x16_bf16 v[32:47], v[104:107], v[116:119], v[32:47]\n\t" \
  "ds_read_b64_tr_b16 v[92:93], v135 offset:20480\n\t" \
  "ds_read_b64_tr_b16 v[94:95], v135 offset:22528\n\t" \
  "v_mfma_f32_32x32x16_bf16 v[48:63], v[104:107], v[120:123], v[48:63]\n\t" \
  "ds_read_b64_tr_b16 v[96:97], v136 offset:20480\n\t" \
  "ds_read_b64_tr_b16 v[98:99], v136 offset:22528\n\t" \
  "s_waitcnt lgkmcnt(9)\n\t" \
  "v_mfma_f32_32x32x16_bf16 v[64:79], v[124:127], v[104:107], v[64:79]\n\t" \
  "ds_read_b64_tr_b16 v[100:101], v137 offset:20480\n\t" \
  "ds_read_b64_tr_b16 v[102:103], v137 offset:20992\n\t" \
  "s_waitcnt lgkmcnt(6)\n\t" \
  "v_mfma_f32_32x32x16_bf16 v[0:15], v[80:83], v[84:87], v[0:15]\n\t" \
  "ds_read_b128 v[104:107], v139 offset:32768\n\t" \
  "ds_read_b64_tr_b16 v[108:109], v133 offset:28672\n\t" \
  "ds_read_b64_tr_b16 v[110:111], v133 offset:30720\n\t" \
  "v_mfma_f32_32x32x16_bf16 v[16:31], v[80:83], v[88:91], v[16:31]\n\t" \
  "ds_read_b64_tr_b16 v[112:113], v134 offset:28672\n\t" \
  "ds_read_b64_tr_b16 v[114:115], v134 offset:30720\n\t" \
  "s_waitcnt lgkmcnt(7)\n\t" \
  "v_mfma_f32_32x32x16_bf16 v[32:47], v[80:83], v[92:95], v[32:47]\n\t" \
  "ds_read_b64_tr_b16 v[116:117], v135 offset:28672\n\t" \
  "ds_read_b64_tr_b16 v[118:119], v135 offset:30720\n\t" \
  "v_mfma_f32_32x32x16_bf16 v[48:63], v[80:83], v[96:99], v[48:63]\n\t" \
  "ds_read_b64_tr_b16 v[120:121], v136 offset:28672\n\t" \
  "ds_read_b64_tr_b16 v[122:123], v136 offset:30720\n\t" \
  "s_waitcnt lgkmcnt(9)\n\t" \
  "v_mfma_f32_32x32x16_bf16 v[64:79], v[100:103], v[80:83], v[64:79]\n\t" \
  "ds_read_b64_tr_b16 v[124:125], v137 offset:22528\n\t" \
  "ds_read_b64_tr_b16 v[126:127], v137 offset:23040\n\t" \
  "s_waitcnt lgkmcnt(6)\n\t" \
  "v_mfma_f32_32x32x16_bf16 v[0:15], v[104:107], v[108:111], v[0:15]\n\t" \
  "s_barrier\n\t" \
  "ds_read_b128 v[80:83], v140 offset:32768\n\t" \
  "ds_read_b64_tr_b16 v[84:85], v128 offset:0\n\t" \
  "ds_read_b64_tr_b16 v[86:87], v128 offset:2048\n\t" \
  "v_mfma_f32_32x32x16_bf16 v[16:31], v[104:107], v[112:115], v[16:31]\n\t" \
  "ds_read_b64_tr_b16 v[88:89], v129 offset:0\n\t" \
  "ds_read_b64_tr_b16 v[90:91], v129 offset:2048\n\t" \
  "s_waitcnt lgkmcnt(7)\n\t" \
  "v_mfma_f32_32x32x16_bf16 v[32:47], v[104:107], v[116:119], v[32:47]\n\t" \
  "ds_read_b64_tr_b16 v[92:93], v130 offset:0\n\t" \
  "ds_read_b64_tr_b16 v[94:95], v130 offset:2048\n\t" \
  "v_mfma_f32_32x32x16_bf16 v[48:63], v[104:107], v[120:123], v[48:63]\n\t" \
  "ds_read_b64_tr_b16 v[96:97], v131 offset:0\n\t" \
  "ds_read_b64_tr_b16 v[98:99], v131 offset:2048\n\t" \
  "s_waitcnt lgkmcnt(9)\n\t" \
  "v_mfma_f32_32x32x16_bf16 v[64:79], v[124:127], v[104:107], v[64:79]\n\t" \
  "ds_read_b64_tr_b16 v[100:101], v132 offset:0\n\t" \
  "ds_read_b64_tr_b16 v[102:103], v132 offset:512\n\t" \
  "s_waitcnt lgkmcnt(6)\n\t" \
  "v_mfma_f32_32x32x16_bf16 v[0:15], v[80:83], v[84:87], v[0:15]\n\t" \
  "ds_read_b128 v[104:107], v141 offset:32768\n\t" \
  "ds_read_b64_tr_b16 v[108:109], v128 offset:8192\n\t" \
  "ds_read_b64_tr_b16 v[110:111], v128 offset:10240\n\t" \
  "v_mfma_f32_32x32x16_bf16 v[16:31], v[80:83], v[88:91], v[16:31]\n\t" \
  "ds_read_b64_tr_b16 v[112:113], v129 offset:8192\n\t" \
  "ds_read_b64_tr_b16 v[114:115], v129 offset:10240\n\t" \
  "s_waitcnt lgkmcnt(7)\n\t" \
  "v_mfma_f32_32x32x16_bf16 v[32:47], v[80:83], v[92:95], v[32:47]\n\t" \
  "ds_read_b64_tr_b16 v[116:117], v130 offset:8192\n\t" \
  "ds_read_b64_tr_b16 v[118:119], v130 offset:10240\n\t" \
  "v_mfma_f32_32x32x16_bf16 v[48:63], v[80:83], v[96:99], v[48:63]\n\t" \
  "ds_read_b64_tr_b16 v[120:121], v131 offset:8192\n\t" \
  "ds_read_b64_tr_b16 v[122:123], v131 offset:10240\n\t" \
  "s_waitcnt lgkmcnt(9)\n\t" \
  "v_mfma_f32_32x32x16_bf16 v[64:79], v[100:103], v[80:83], v[64:79]\n\t" \
  "ds_read_b64_tr_b16 v[124:125], v132 offset:2048\n\t" \
  "ds_read_b64_tr_b16 v[126:127], v132 offset:2560\n\t" \
  "s_waitcnt lgkmcnt(6)\n\t" \
  "v_mfma_f32_32x32x16_bf16 v[0:15], v[104:107], v[108:111], v[0:15]\n\t" \
  "s_barrier\n\t" \
  "ds_read_b128 v[80:83], v138 offset:0\n\t" \
  "ds_read_b64_tr_b16 v[84:85], v128 offset:20480\n\t" \
  "ds_read_b64_tr_b16 v[86:87], v128 offset:22528\n\t" \
  "v_mfma_f32_32x32x16_bf16 v[16:31], v[104:107], v[112:115], v[16:31]\n\t" \
  "ds_read_b64_tr_b16 v[88:89], v129 offset:20480\n\t" \
  "ds_read_b64_tr_b16 v[90:91], v129 offset:22528\n\t" \
  "s_waitcnt lgkmcnt(7)\n\t" \
  "v_mfma_f32_32x32x16_bf16 v[32:47], v[104:107], v[116:119], v[32:47]\n\t" \
  "ds_read_b64_tr_b16 v[92:93], v130 offset:20480\n\t" \
  "ds_read_b64_tr_b16 v[94:95], v130 offset:22528\n\t" \
  "v_mfma_f32_32x32x16_bf16 v[48:63], v[104:107], v[120:123], v[48:63]\n\t" \
  "ds_read_b64_tr_b16 v[96:97], v131 offset:20480\n\t" \
  "ds_read_b64_tr_b16 v[98:99], v131 offset:22528\n\t" \
  "s_waitcnt lgkmcnt(9)\n\t" \
  "v_mfma_f32_32x32x16_bf16 v[64:79], v[124:127], v[104:107], v[64:79]\n\t" \
  "ds_read_b64_tr_b16 v[100:101], v132 offset:20480\n\t" \
  "ds_read_b64_tr_b16 v[102:103], v132 offset:20992\n\t" \
  "s_waitcnt lgkmcnt(6)\n\t" \
  "v_mfma_f32_32x32x16_bf16 v[0:15], v[80:83], v[84:87], v[0:15]\n\t" \
  "ds_read_b128 v[104:107], v139 offset:0\n\t" \
  "ds_read_b64_tr_b16 v[108:109], v128 offset:28672\n\t" \
  "ds_read_b64_tr_b16 v[110:111], v128 offset:30720\n\t" \
  "v_mfma_f32_32x32x16_bf16 v[16:31], v[80:83], v[88:91], v[16:31]\n\t" \
  "ds_read_b64_tr_b16 v[112:113], v129 offset:28672\n\t" \
  "ds_read_b64_tr_b16 v[114:115], v129 offset:30720\n\t" \
  "s_waitcnt lgkmcnt(7)\n\t" \
  "v_mfma_f32_32x32x16_bf16 v[32:47], v[80:83], v[92:95], v[32:47]\n\t" \
  "ds_read_b64_tr_b16 v[116:117], v130 offset:28672\n\t" \
  "ds_read_b64_tr_b16 v[118:119], v130 offset:30720\n\t" \
  "v_mfma_f32_32x32x16_bf16 v[48:63], v[80:83], v[96:99], v[48:63]\n\t" \
  "ds_read_b64_tr_b16 v[120:121], v131 offset:28672\n\t" \
  "ds_read_b64_tr_b16 v[122:123], v131 offset:30720\n\t" \
  "s_waitcnt lgkmcnt(9)\n\t" \
  "v_mfma_f32_32x32x16_bf16 v[64:79], v[100:103], v[80:83], v[64:79]\n\t" \
  "ds_read_b64_tr_b16 v[124:125], v132 offset:22528\n\t" \
  "ds_read_b64_tr_b16 v[126:127], v132 offset:23040\n\t" \
  "s_waitcnt lgkmcnt(6)\n\t" \
  "v_mfma_f32_32x32x16_bf16 v[0:15], v[104:107], v[108:111], v[0:15]\n\t" \
  "s_barrier\n\t" \
  "ds_read_b128 v[80:83], v140 offset:0\n\t" \
  "ds_read_b64_tr_b16 v[84:85], v128 offset:40960\n\t" \
  "ds_read_b64_tr_b16 v[86:87], v128 offset:43008\n\t" \
  "v_mfma_f32_32x32x16_bf16 v[16:31], v[104:107], v[112:115], v[16:31]\n\t" \
  "ds_read_b64_tr_b16 v[88:89], v129 offset:40960\n\t" \
  "ds_read_b64_tr_b16 v[90:91], v129 offset:43008\n\t" \
  "s_waitcnt lgkmcnt(7)\n\t" \
  "v_mfma_f32_32x32x16_bf16 v[32:47], v[104:107], v[116:119], v[32:47]\n\t" \
  "ds_read_b64_tr_b16 v[92:93], v130 offset:40960\n\t" \
  "ds_read_b64_tr_b16 v[94:95], v130 offset:43008\n\t" \
  "v_mfma_f32_32x32x16_bf16 v[48:63], v[104:107], v[120:123], v[48:63]\n\t" \
  "ds_read_b64_tr_b16 v[96:97], v131 offset:40960\n\t" \
  "ds_read_b64_tr_b16 v[98:99], v131 offset:43008\n\t" \
  "s_waitcnt lgkmcnt(9)\n\t" \
  "v_mfma_f32_32x32x16_bf16 v[64:79], v[124:127], v[104:107], v[64:79]\n\t" \
  "ds_read_b64_tr_b16 v[100:101], v132 offset:40960\n\t" \
  "ds_read_b64_tr_b16 v[102:103], v132 offset:41472\n\t" \
  "s_waitcnt lgkmcnt(6)\n\t" \
  "v_mfma_f32_32x32x16_bf16 v[0:15], v[80:83], v[84:87], v[0:15]\n\t" \
  "ds_read_b128 v[104:107], v141 offset:0\n\t" \
  "ds_read_b64_tr_b16 v[108:109], v128 offset:49152\n\t" \
  "ds_read_b64_tr_b16 v[110:111], v128 offset:51200\n\t" \
  "v_mfma_f32_32x32x16_bf16 v[16:31], v[80:83], v[88:91], v[16:31]\n\t" \
  "ds_read_b64_tr_b16 v[112:113], v129 offset:49152\n\t" \
  "ds_read_b64_tr_b16 v[114:115], v129 offset:51200\n\t" \
  "s_waitcnt lgkmcnt(7)\n\t" \
  "v_mfma_f32_32x32x16_bf16 v[32:47], v[80:83], v[92:95], v[32:47]\n\t" \
  "ds_read_b64_tr_b16 v[116:117], v130 offset:49152\n\t" \
  "ds_read_b64_tr_b16 v[118:119], v130 offset:51200\n\t" \
  "v_mfma_f32_32x32x16_bf16 v[48:63], v[80:83], v[96:99], v[48:63]\n\t" \
  "ds_read_b64_tr_b16 v[120:121], v131 offset:49152\n\t" \
  "ds_read_b64_tr_b16 v[122:123], v131 offset:51200\n\t" \
  "s_waitcnt lgkmcnt(9)\n\t" \
  "v_mfma_f32_32x32x16_bf16 v[64:79], v[100:103], v[80:83], v[64:79]\n\t" \
  "ds_read_b64_tr_b16 v[124:125], v132 offset:43008\n\t" \
  "ds_read_b64_tr_b16 v[126:127], v132 offset:43520\n\t" \
  "s_waitcnt lgkmcnt(6)\n\t" \
  "v_mfma_f32_32x32x16_bf16 v[0:15], v[104:107], v[108:111], v[0:15]\n\t" \
  "s_barrier\n\t" \
  "ds_read_b128 v[80:83], v138 offset:16384\n\t" \
  "ds_read_b64_tr_b16 v[84:85], v133 offset:0\n\t" \
  "ds_read_b64_tr_b16 v[86:87], v133 offset:2048\n\t" \
  "v_mfma_f32_32x32x16_bf16 v[16:31], v[104:107], v[112:115], v[16:31]\n\t" \
  "ds_read_b64_tr_b16 v[88:89], v134 offset:0\n\t" \
  "ds_read_b64_tr_b16 v[90:91], v134 offset:2048\n\t" \
  "s_waitcnt lgkmcnt(7)\n\t" \
  "v_mfma_f32_32x32x16_bf16 v[32:47], v[104:107], v[116:119], v[32:47]\n\t" \
  "ds_read_b64_tr_b16 v[92:93], v135 offset:0\n\t" \
  "ds_read_b64_tr_b16 v[94:95], v135 offset:2048\n\t" \
  "v_mfma_f32_32x32x16_bf16 v[48:63], v[104:107], v[120:123], v[48:63]\n\t" \
  "ds_read_b64_tr_b16 v[96:97], v136 offset:0\n\t" \
  "ds_read_b64_tr_b16 v[98:99], v136 offset:2048\n\t" \
  "s_waitcnt lgkmcnt(9)\n\t" \
  "v_mfma_f32_32x32x16_bf16 v[64:79], v[124:127], v[104:107], v[64:79]\n\t" \
  "ds_read_b64_tr_b16 v[100:101], v137 offset:0\n\t" \
  "ds_read_b64_tr_b16 v[102:103], v137 offset:512\n\t" \
  "s_waitcnt lgkmcnt(6)\n\t" \
  "v_mfma_f32_32x32x16_bf16 v[0:15], v[80:83], v[84:87], v[0:15]\n\t" \
  "ds_read_b128 v[104:107], v139 offset:16384\n\t" \
  "ds_read_b64_tr_b16 v[108:109], v133 offset:8192\n\t" \
  "ds_read_b64_tr_b16 v[110:111], v133 offset:10240\n\t" \
  "v_mfma_f32_32x32x16_bf16 v[16:31], v[80:83], v[88:91], v[16:31]\n\t" \
  "ds_read_b64_tr_b16 v[112:113], v134 offset:8192\n\t" \
  "ds_read_b64_tr_b16 v[114:115], v134 offset:10240\n\t" \
  "s_waitcnt lgkmcnt(7)\n\t" \
  "v_mfma_f32_32x32x16_bf16 v[32:47], v[80:83], v[92:95], v[32:47]\n\t" \
  "ds_read_b64_tr_b16 v[116:117], v135 offset:8192\n\t" \
  "ds_read_b64_tr_b16 v[118:119], v135 offset:10240\n\t" \
  "v_mfma_f32_32x32x16_bf16 v[48:63], v[80:83], v[96:99], v[48:63]\n\t" \
  "ds_read_b64_tr_b16 v[120:121], v136 offset:8192\n\t" \
  "ds_read_b64_tr_b16 v[122:123], v136 offset:10240\n\t" \
  "s_waitcnt lgkmcnt(9)\n\t" \
  "v_mfma_f32_32x32x16_bf16 v[64:79], v[100:103], v[80:83], v[64:79]\n\t" \
  "ds_read_b64_tr_b16 v[124:125], v137 offset:2048\n\t" \
  "ds_read_b64_tr_b16 v[126:127], v137 offset:2560\n\t" \
  "s_waitcnt lgkmcnt(6)\n\t" \
  "v_mfma_f32_32x32x16_bf16 v[0:15], v[104:107], v[108:111], v[0:15]\n\t" \
  "s_barrier\n\t" \
  "ds_read_b128 v[80:83], v140 offset:16384\n\t" \
  "ds_read_b64_tr_b16 v[84:85], v133 offset:20480\n\t" \
  "ds_read_b64_tr_b16 v[86:87], v133 offset:22528\n\t" \
  "v_mfma_f32_32x32x16_bf16 v[16:31], v[104:107], v[112:115], v[16:31]\n\t" \
  "ds_read_b64_tr_b16 v[88:89], v134 offset:20480\n\t" \
  "ds_read_b64_tr_b16 v[90:91], v134 offset:22528\n\t" \
  "s_waitcnt lgkmcnt(7)\n\t" \
  "v_mfma_f32_32x32x16_bf16 v[32:47], v[104:107], v[116:119], v[32:47]\n\t" \
  "ds_read_b64_tr_b16 v[92:93], v135 offset:20480\n\t" \
  "ds_read_b64_tr_b16 v[94:95], v135 offset:22528\n\t" \
  "v_mfma_f32_32x32x16_bf16 v[48:63], v[104:107], v[120:123], v[48:63]\n\t" \
  "ds_read_b64_tr_b16 v[96:97], v136 offset:20480\n\t" \
  "ds_read_b64_tr_b16 v[98:99], v136 offset:22528\n\t" \
  "s_waitcnt lgkmcnt(9)\n\t" \
  "v_mfma_f32_32x32x16_bf16 v[64:79], v[124:127], v[104:107], v[64:79]\n\t" \
  "ds_read_b64_tr_b16 v[100:101], v137 offset:20480\n\t" \
  "ds_read_b64_tr_b16 v[102:103], v137 offset:20992\n\t" \
  "s_waitcnt lgkmcnt(6)\n\t" \
  "v_mfma_f32_32x32x16_bf16 v[0:15], v[80:83], v[84:87], v[0:15]\n\t" \
  "ds_read_b128 v[104:107], v141 offset:16384\n\t" \
  "ds_read_b64_tr_b16 v[108:109], v133 offset:28672\n\t" \
  "ds_read_b64_tr_b16 v[110:111], v133 offset:30720\n\t" \
  "v_mfma_f32_32x32x16_bf16 v[16:31], v[80:83], v[88:91], v[16:31]\n\t" \
  "ds_read_b64_tr_b16 v[112:113], v134 offset:28672\n\t" \
  "ds_read_b64_tr_b16 v[114:115], v134 offset:30720\n\t" \
  "s_waitcnt lgkmcnt(7)\n\t" \
  "v_mfma_f32_32x32x16_bf16 v[32:47], v[80:83], v[92:95], v[32:47]\n\t" \
  "ds_read_b64_tr_b16 v[116:117], v135 offset:28672\n\t" \
  "ds_read_b64_tr_b16 v[118:119], v135 offset:30720\n\t" \
  "v_mfma_f32_32x32x16_bf16 v[48:63], v[80:83], v[96:99], v[48:63]\n\t" \
  "ds_read_b64_tr_b16 v[120:121], v136 offset:28672\n\t" \
  "ds_read_b64_tr_b16 v[122:123], v136 offset:30720\n\t" \
  "s_waitcnt lgkmcnt(9)\n\t" \
  "v_mfma_f32_32x32x16_bf16 v[64:79], v[100:103], v[80:83], v[64:79]\n\t" \
  "ds_read_b64_tr_b16 v[124:125], v137 offset:22528\n\t" \
  "ds_read_b64_tr_b16 v[126:127], v137 offset:23040\n\t" \
  "s_waitcnt lgkmcnt(6)\n\t" \
  "v_mfma_f32_32x32x16_bf16 v[0:15], v[104:107], v[108:111], v[0:15]\n\t" \
  "s_barrier\n\t" \
  "ds_read_b128 v[80:83], v138 offset:32768\n\t" \
  "ds_read_b64_tr_b16 v[84:85], v128 offset:0\n\t" \
  "ds_read_b64_tr_b16 v[86:87], v128 offset:2048\n\t" \
  "v_mfma_f32_32x32x16_bf16 v[16:31], v[104:107], v[112:115], v[16:31]\n\t" \
  "ds_read_b64_tr_b16 v[88:89], v129 offset:0\n\t" \
  "ds_read_b64_tr_b16 v[90:91], v129 offset:2048\n\t" \
  "s_waitcnt lgkmcnt(7)\n\t" \
  "v_mfma_f32_32x32x16_bf16 v[32:47], v[104:107], v[116:119], v[32:47]\n\t" \
  "ds_read_b64_tr_b16 v[92:93], v130 offset:0\n\t" \
  "ds_read_b64_tr_b16 v[94:95], v130 offset:2048\n\t" \
  "v_mfma_f32_32x32x16_bf16 v[48:63], v[104:107], v[120:123], v[48:63]\n\t" \
  "ds_read_b64_tr_b16 v[96:97], v131 offset:0\n\t" \
  "ds_read_b64_tr_b16 v[98:99], v131 offset:2048\n\t" \
  "s_waitcnt lgkmcnt(9)\n\t" \
  "v_mfma_f32_32x32x16_bf16 v[64:79], v[124:127], v[104:107], v[64:79]\n\t" \
  "ds_read_b64_tr_b16 v[100:101], v132 offset:0\n\t" \
  "ds_read_b64_tr_b16 v[102:103], v132 offset:512\n\t" \
  "s_waitcnt lgkmcnt(6)\n\t" \
  "v_mfma_f32_32x32x16_bf16 v[0:15], v[80:83], v[84:87], v[0:15]\n\t" \
  "ds_read_b128 v[104:107], v139 offset:32768\n\t" \
  "ds_read_b64_tr_b16 v[108:109], v128 offset:8192\n\t" \
  "ds_read_b64_tr_b16 v[110:111], v128 offset:10240\n\t" \
  "v_mfma_f32_32x32x16_bf16 v[16:31], v[80:83], v[88:91], v[16:31]\n\t" \
  "ds_read_b64_tr_b16 v[112:113], v129 offset:8192\n\t" \
  "ds_read_b64_tr_b16 v[114:115], v129 offset:10240\n\t" \
  "s_waitcnt lgkmcnt(7)\n\t" \
  "v_mfma_f32_32x32x16_bf16 v[32:47], v[80:83], v[92:95], v[32:47]\n\t" \
  "ds_read_b64_tr_b16 v[116:117], v130 offset:8192\n\t" \
  "ds_read_b64_tr_b16 v[118:119], v130 offset:10240\n\t" \
  "v_mfma_f32_32x32x16_bf16 v[48:63], v[80:83], v[96:99], v[48:63]\n\t" \
  "ds_read_b64_tr_b16 v[120:121], v131 offset:8192\n\t" \
  "ds_read_b64_tr_b16 v[122:123], v131 offset:10240\n\t" \
  "s_waitcnt lgkmcnt(9)\n\t" \
  "v_mfma_f32_32x32x16_bf16 v[64:79], v[100:103], v[80:83], v[64:79]\n\t" \
  "ds_read_b64_tr_b16 v[124:125], v132 offset:2048\n\t" \
  "ds_read_b64_tr_b16 v[126:127], v132 offset:2560\n\t" \
  "s_waitcnt lgkmcnt(6)\n\t" \
  "v_mfma_f32_32x32x16_bf16 v[0:15], v[104:107], v[108:111], v[0:15]\n\t" \
  "s_barrier\n\t" \
  "ds_read_b128 v[80:83], v140 offset:32768\n\t" \
  "ds_read_b64_tr_b16 v[84:85], v128 offset:20480\n\t" \
  "ds_read_b64_tr_b16 v[86:87], v128 offset:22528\n\t" \
  "v_mfma_f32_32x32x16_bf16 v[16:31], v[104:107], v[112:115], v[16:31]\n\t" \
  "ds_read_b64_tr_b16 v[88:89], v129 offset:20480\n\t" \
  "ds_read_b64_tr_b16 v[90:91], v129 offset:22528\n\t" \
  "s_waitcnt lgkmcnt(7)\n\t" \
  "v_mfma_f32_32x32x16_bf16 v[32:47], v[104:107], v[116:119], v[32:47]\n\t" \
  "ds_read_b64_tr_b16 v[92:93], v130 offset:20480\n\t" \
  "ds_read_b64_tr_b16 v[94:95], v130 offset:22528\n\t" \
  "v_mfma_f32_32x32x16_bf16 v[48:63], v[104:107], v[120:123], v[48:63]\n\t" \
  "ds_read_b64_tr_b16 v[96:97], v131 offset:20480\n\t" \
  "ds_read_b64_tr_b16 v[98:99], v131 offset:22528\n\t" \
  "s_waitcnt lgkmcnt(9)\n\t" \
  "v_mfma_f32_32x32x16_bf16 v[64:79], v[124:127], v[104:107], v[64:79]\n\t" \
  "ds_read_b64_tr_b16 v[100:101], v132 offset:20480\n\t" \
  "ds_read_b64_tr_b16 v[102:103], v132 offset:20992\n\t" \
  "s_waitcnt lgkmcnt(6)\n\t" \
  "v_mfma_f32_32x32x16_bf16 v[0:15], v[80:83], v[84:87], v[0:15]\n\t" \
  "ds_read_b128 v[104:107], v141 offset:32768\n\t" \
  "ds_read_b64_tr_b16 v[108:109], v128 offset:28672\n\t" \
  "ds_read_b64_tr_b16 v[110:111], v128 offset:30720\n\t" \
  "v_mfma_f32_32x32x16_bf16 v[16:31], v[80:83], v[88:91], v[16:31]\n\t" \
  "ds_read_b64_tr_b16 v[112:113], v129 offset:28672\n\t" \
  "ds_read_b64_tr_b16 v[114:115], v129 offset:30720\n\t" \
  "s_waitcnt lgkmcnt(7)\n\t" \
  "v_mfma_f32_32x32x16_bf16 v[32:47], v[80:83], v[92:95], v[32:47]\n\t" \
  "ds_read_b64_tr_b16 v[116:117], v130 offset:28672\n\t" \
  "ds_read_b64_tr_b16 v[118:119], v130 offset:30720\n\t" \
  "v_mfma_f32_32x32x16_bf16 v[48:63], v[80:83], v[96:99], v[48:63]\n\t" \
  "ds_read_b64_tr_b16 v[120:121], v131 offset:28672\n\t" \
  "ds_read_b64_tr_b16 v[122:123], v131 offset:30720\n\t" \
  "s_waitcnt lgkmcnt(9)\n\t" \
  "v_mfma_f32_32x32x16_bf16 v[64:79], v[100:103], v[80:83], v[64:79]\n\t" \
  "ds_read_b64_tr_b16 v[124:125], v132 offset:22528\n\t" \
  "ds_read_b64_tr_b16 v[126:127], v132 offset:23040\n\t" \
  "s_waitcnt lgkmcnt(6)\n\t" \
  "v_mfma_f32_32x32x16_bf16 v[0:15], v[104:107], v[108:111], v[0:15]\n\t" \
  "s_barrier\n\t" \
  "ds_read_b128 v[80:83], v138 offset:0\n\t" \
  "ds_read_b64_tr_b16 v[84:85], v128 offset:40960\n\t" \
  "ds_read_b64_tr_b16 v[86:87], v128 offset:43008\n\t" \
  "v_mfma_f32_32x32x16_bf16 v[16:31], v[104:107], v[112:115], v[16:31]\n\t" \
  "ds_read_b64_tr_b16 v[88:89], v129 offset:40960\n\t" \
  "ds_read_b64_tr_b16 v[90:91], v129 offset:43008\n\t" \
  "s_waitcnt lgkmcnt(7)\n\t" \
  "v_mfma_f32_32x32x16_bf16 v[32:47], v[104:107], v[116:119], v[32:47]\n\t" \
  "ds_read_b64_tr_b16 v[92:93], v130 offset:40960\n\t" \
  "ds_read_b64_tr_b16 v[94:95], v130 offset:43008\n\t" \
  "v_mfma_f32_32x32x16_bf16 v[48:63], v[104:107], v[120:123], v[48:63]\n\t" \
  "ds_read_b64_tr_b16 v[96:97], v131 offset:40960\n\t" \
  "ds_read_b64_tr_b16 v[98:99], v131 offset:43008\n\t" \
  "s_waitcnt lgkmcnt(9)\n\t" \
  "v_mfma_f32_32x32x16_bf16 v[64:79], v[124:127], v[104:107], v[64:79]\n\t" \
  "ds_read_b64_tr_b16 v[100:101], v132 offset:40960\n\t" \
  "ds_read_b64_tr_b16 v[102:103], v132 offset:41472\n\t" \
  "s_waitcnt lgkmcnt(6)\n\t" \
  "v_mfma_f32_32x32x16_bf16 v[0:15], v[80:83], v[84:87], v[0:15]\n\t" \
  "ds_read_b128 v[104:107], v139 offset:0\n\t" \
  "ds_read_b64_tr_b16 v[108:109], v128 offset:49152\n\t" \
  "ds_read_b64_tr_b16 v[110:111], v128 offset:51200\n\t" \
  "v_mfma_f32_32x32x16_bf16 v[16:31], v[80:83], v[88:91], v[16:31]\n\t" \
  "ds_read_b64_tr_b16 v[112:113], v129 offset:49152\n\t" \
  "ds_read_b64_tr_b16 v[114:115], v129 offset:51200\n\t" \
  "s_waitcnt lgkmcnt(7)\n\t" \
  "v_mfma_f32_32x32x16_bf16 v[32:47], v[80:83], v[92:95], v[32:47]\n\t" \
  "ds_read_b64_tr_b16 v[116:117], v130 offset:49152\n\t" \
  "ds_read_b64_tr_b16 v[118:119], v130 offset:51200\n\t" \
  "v_mfma_f32_32x32x16_bf16 v[48:63], v[80:83], v[96:99], v[48:63]\n\t" \
  "ds_read_b64_tr_b16 v[120:121], v131 offset:49152\n\t" \
  "ds_read_b64_tr_b16 v[122:123], v131 offset:51200\n\t" \
  "s_waitcnt lgkmcnt(9)\n\t" \
  "v_mfma_f32_32x32x16_bf16 v[64:79], v[100:103], v[80:83], v[64:79]\n\t" \
  "ds_read_b64_tr_b16 v[124:125], v132 offset:43008\n\t" \
  "ds_read_b64_tr_b16 v[126:127], v132 offset:43520\n\t" \
  "s_waitcnt lgkmcnt(6)\n\t" \
  "v_mfma_f32_32x32x16_bf16 v[0:15], v[104:107], v[108:111], v[0:15]\n\t" \
  "s_barrier\n\t" \
  "ds_read_b128 v[80:83], v140 offset:0\n\t" \
  "ds_read_b64_tr_b16 v[84:85], v133 offset:0\n\t" \
  "ds_read_b64_tr_b16 v[86:87], v133 offset:2048\n\t" \
  "v_mfma_f32_32x32x16_bf16 v[16:31], v[104:107], v[112:115], v[16:31]\n\t" \
  "ds_read_b64_tr_b16 v[88:89], v134 offset:0\n\t" \
  "ds_read_b64_tr_b16 v[90:91], v134 offset:2048\n\t" \
  "s_waitcnt lgkmcnt(7)\n\t" \
  "v_mfma_f32_32x32x16_bf16 v[32:47], v[104:107], v[116:119], v[32:47]\n\t" \
  "ds_read_b64_tr_b16 v[92:93], v135 offset:0\n\t" \
  "ds_read_b64_tr_b16 v[94:95], v135 offset:2048\n\t" \
  "v_mfma_f32_32x32x16_bf16 v[48:63], v[104:107], v[120:123], v[48:63]\n\t" \
  "ds_read_b64_tr_b16 v[96:97], v136 offset:0\n\t" \
  "ds_read_b64_tr_b16 v[98:99], v136 offset:2048\n\t" \
  "s_waitcnt lgkmcnt(9)\n\t" \
  "v_mfma_f32_32x32x16_bf16 v[64:79], v[124:127], v[104:107], v[64:79]\n\t" \
  "ds_read_b64_tr_b16 v[100:101], v137 offset:0\n\t" \
  "ds_read_b64_tr_b16 v[102:103], v137 offset:512\n\t" \
  "s_waitcnt lgkmcnt(6)\n\t" \
  "v_mfma_f32_32x32x16_bf16 v[0:15], v[80:83], v[84:87], v[0:15]\n\t" \
  "ds_read_b128 v[104:107], v141 offset:0\n\t" \
  "ds_read_b64_tr_b16 v[108:109], v133 offset:8192\n\t" \
  "ds_read_b64_tr_b16 v[110:111], v133 offset:10240\n\t" \
  "v_mfma_f32_32x32x16_bf16 v[16:31], v[80:83], v[88:91], v[16:31]\n\t" \
  "ds_read_b64_tr_b16 v[112:113], v134 offset:8192\n\t" \
  "ds_read_b64_tr_b16 v[114:115], v134 offset:10240\n\t" \
  "s_waitcnt lgkmcnt(7)\n\t" \
  "v_mfma_f32_32x32x16_bf16 v[32:47], v[80:83], v[92:95], v[32:47]\n\t" \
  "ds_read_b64_tr_b16 v[116:117], v135 offset:8192\n\t" \
  "ds_read_b64_tr_b16 v[118:119], v135 offset:10240\n\t" \
  "v_mfma_f32_32x32x16_bf16 v[48:63], v[80:83], v[96:99], v[48:63]\n\t" \
  "ds_read_b64_tr_b16 v[120:121], v136 offset:8192\n\t" \
  "ds_read_b64_tr_b16 v[122:123], v136 offset:10240\n\t" \
  "s_waitcnt lgkmcnt(9)\n\t" \
  "v_mfma_f32_32x32x16_bf16 v[64:79], v[100:103], v[80:83], v[64:79]\n\t" \
  "ds_read_b64_tr_b16 v[124:125], v137 offset:2048\n\t" \
  "ds_read_b64_tr_b16 v[126:127], v137 offset:2560\n\t" \
  "s_waitcnt lgkmcnt(6)\n\t" \
  "v_mfma_f32_32x32x16_bf16 v[0:15], v[104:107], v[108:111], v[0:15]\n\t" \
  "s_barrier\n\t" \
  "ds_read_b128 v[80:83], v138 offset:16384\n\t" \
  "ds_read_b64_tr_b16 v[84:85], v133 offset:20480\n\t" \
  "ds_read_b64_tr_b16 v[86:87], v133 offset:22528\n\t" \
  "v_mfma_f32_32x32x16_bf16 v[16:31], v[104:107], v[112:115], v[16:31]\n\t" \
  "ds_read_b64_tr_b16 v[88:89], v134 offset:20480\n\t" \
  "ds_read_b64_tr_b16 v[90:91], v134 offset:22528\n\t" \
  "s_waitcnt lgkmcnt(7)\n\t" \
  "v_mfma_f32_32x32x16_bf16 v[32:47], v[104:107], v[116:119], v[32:47]\n\t" \
  "ds_read_b64_tr_b16 v[92:93], v135 offset:20480\n\t" \
  "ds_read_b64_tr_b16 v[94:95], v135 offset:22528\n\t" \
  "v_mfma_f32_32x32x16_bf16 v[48:63], v[104:107], v[120:123], v[48:63]\n\t" \
  "ds_read_b64_tr_b16 v[96:97], v136 offset:20480\n\t" \
  "ds_read_b64_tr_b16 v[98:99], v136 offset:22528\n\t" \
  "s_waitcnt lgkmcnt(9)\n\t" \
  "v_mfma_f32_32x32x16_bf16 v[64:79], v[124:127], v[104:107], v[64:79]\n\t" \
  "ds_read_b64_tr_b16 v[100:101], v137 offset:20480\n\t" \
  "ds_read_b64_tr_b16 v[102:103], v137 offset:20992\n\t" \
  "s_waitcnt lgkmcnt(6)\n\t" \
  "v_mfma_f32_32x32x16_bf16 v[0:15], v[80:83], v[84:87], v[0:15]\n\t" \
  "ds_read_b128 v[104:107], v139 offset:16384\n\t" \
  "ds_read_b64_tr_b16 v[108:109], v133 offset:28672\n\t" \
  "ds_read_b64_tr_b16 v[110:111], v133 offset:30720\n\t" \
  "v_mfma_f32_32x32x16_bf16 v[16:31], v[80:83], v[88:91], v[16:31]\n\t" \
  "ds_read_b64_tr_b16 v[112:113], v134 offset:28672\n\t" \
  "ds_read_b64_tr_b16 v[114:115], v134 offset:30720\n\t" \
  "s_waitcnt lgkmcnt(7)\n\t" \
  "v_mfma_f32_32x32x16_bf16 v[32:47], v[80:83], v[92:95], v[32:47]\n\t" \
  "ds_read_b64_tr_b16 v[116:117], v135 offset:28672\n\t" \
  "ds_read_b64_tr_b16 v[118:119], v135 offset:30720\n\t" \
  "v_mfma_f32_32x32x16_bf16 v[48:63], v[80:83], v[96:99], v[48:63]\n\t" \
  "ds_read_b64_tr_b16 v[120:121], v136 offset:28672\n\t" \
  "ds_read_b64_tr_b16 v[122:123], v136 offset:30720\n\t" \
  "s_waitcnt lgkmcnt(9)\n\t" \
  "v_mfma_f32_32x32x16_bf16 v[64:79], v[100:103], v[80:83], v[64:79]\n\t" \
  "ds_read_b64_tr_b16 v[124:125], v137 offset:22528\n\t" \
  "ds_read_b64_tr_b16 v[126:127], v137 offset:23040\n\t" \
  "s_waitcnt lgkmcnt(6)\n\t" \
  "v_mfma_f32_32x32x16_bf16 v[0:15], v[104:107], v[108:111], v[0:15]\n\t" \
  "s_barrier\n\t" \
  "ds_read_b128 v[80:83], v140 offset:16384\n\t" \
  "ds_read_b64_tr_b16 v[84:85], v128 offset:0\n\t" \
  "ds_read_b64_tr_b16 v[86:87], v128 offset:2048\n\t" \
  "v_mfma_f32_32x32x16_bf16 v[16:31], v[104:107], v[112:115], v[16:31]\n\t" \
  "ds_read_b64_tr_b16 v[88:89], v129 offset:0\n\t" \
  "ds_read_b64_tr_b16 v[90:91], v129 offset:2048\n\t" \
  "s_waitcnt lgkmcnt(7)\n\t" \
  "v_mfma_f32_32x32x16_bf16 v[32:47], v[104:107], v[116:119], v[32:47]\n\t" \
  "ds_read_b64_tr_b16 v[92:93], v130 offset:0\n\t" \
  "ds_read_b64_tr_b16 v[94:95], v130 offset:2048\n\t" \
  "v_mfma_f32_32x32x16_bf16 v[48:63], v[104:107], v[120:123], v[48:63]\n\t" \
  "ds_read_b64_tr_b16 v[96:97], v131 offset:0\n\t" \
  "ds_read_b64_tr_b16 v[98:99], v131 offset:2048\n\t" \
  "s_waitcnt lgkmcnt(9)\n\t" \
  "v_mfma_f32_32x32x16_bf16 v[64:79], v[124:127], v[104:107], v[64:79]\n\t" \
  "ds_read_b64_tr_b16 v[100:101], v132 offset:0\n\t" \
  "ds_read_b64_tr_b16 v[102:103], v132 offset:512\n\t" \
  "s_waitcnt lgkmcnt(6)\n\t" \
  "v_mfma_f32_32x32x16_bf16 v[0:15], v[80:83], v[84:87], v[0:15]\n\t" \
  "ds_read_b128 v[104:107], v141 offset:16384\n\t" \
  "ds_read_b64_tr_b16 v[108:109], v128 offset:8192\n\t" \
  "ds_read_b64_tr_b16 v[110:111], v128 offset:10240\n\t" \
  "v_mfma_f32_32x32x16_bf16 v[16:31], v[80:83], v[88:91], v[16:31]\n\t" \
  "ds_read_b64_tr_b16 v[112:113], v129 offset:8192\n\t" \
  "ds_read_b64_tr_b16 v[114:115], v129 offset:10240\n\t" \
  "s_waitcnt lgkmcnt(7)\n\t" \
  "v_mfma_f32_32x32x16_bf16 v[32:47], v[80:83], v[92:95], v[32:47]\n\t" \
  "ds_read_b64_tr_b16 v[116:117], v130 offset:8192\n\t" \
  "ds_read_b64_tr_b16 v[118:119], v130 offset:10240\n\t" \
  "v_mfma_f32_32x32x16_bf16 v[48:63], v[80:83], v[96:99], v[48:63]\n\t" \
  "ds_read_b64_tr_b16 v[120:121], v131 offset:8192\n\t" \
  "ds_read_b64_tr_b16 v[122:123], v131 offset:10240\n\t" \
  "s_waitcnt lgkmcnt(9)\n\t" \
  "v_mfma_f32_32x32x16_bf16 v[64:79], v[100:103], v[80:83], v[64:79]\n\t" \
  "ds_read_b64_tr_b16 v[124:125], v132 offset:2048\n\t" \
  "ds_read_b64_tr_b16 v[126:127], v132 offset:2560\n\t" \
  "s_waitcnt lgkmcnt(6)\n\t" \
  "v_mfma_f32_32x32x16_bf16 v[0:15], v[104:107], v[108:111], v[0:15]\n\t" \
  "v_mfma_f32_32x32x16_bf16 v[16:31], v[104:107], v[112:115], v[16:31]\n\t" \
  "s_waitcnt lgkmcnt(2)\n\t" \
  "v_mfma_f32_32x32x16_bf16 v[32:47], v[104:107], v[116:119], v[32:47]\n\t" \
  "v_mfma_f32_32x32x16_bf16 v[48:63], v[104:107], v[120:123], v[48:63]\n\t" \
  "s_waitcnt lgkmcnt(0)\n\t" \
  "v_mfma_f32_32x32x16_bf16 v[64:79], v[124:127], v[104:107], v[64:79]\n\t" \
  "s_nop 15\n\t"

constexpr int G1_WS = 20480, G1_WN = 5, G1_XS = 16384, G1_XN = 3;
constexpr int G1_W = 0, G1_X = G1_WN * G1_WS;
constexpr int G1_RS = G1_X + G1_XN * G1_XS;
constexpr int G1_RM = G1_RS + 512;
constexpr int G1_CS = G1_RM + 512;
constexpr int G1_LDS = G1_CS + 320 * 4;

DEV f32x2 splat2(float v) { return f32x2{v, v}; }
DEV f32x2 gelu_mul2(f32x2 g, f32x2 u) {
  const float c1 = -2.0f * 0.7978845608028654f * 1.4426950408889634f;
  const float c2 = c1 * 0.044715f;
  const f32x2 z = g * __builtin_elementwise_fma(g * g, splat2(c2), splat2(c1));
  const f32x2 d = f32x2{__builtin_amdgcn_exp2f(z[0]), __builtin_amdgcn_exp2f(z[1])} + 1.0f;
  return (g * u) * f32x2{__builtin_amdgcn_rcpf(d[0]), __builtin_amdgcn_rcpf(d[1])};
}

DEV int g1_koff(int pi, int cj) {
  const int a = pi >> 3, p = pi & 7, c = cj & 3;
  const int f = STAG == 0 ? 0 : STAG == 1 ? p + 2 * c : STAG == 2 ? ((p + c) & 1) * 2 : STAG == 3 ? ((p + c) & 3) : 2 * ((p >> 1) + c);
  return (4 * a + f) & 7;
}
template <int CTRL> DEV float dpp_add(float v) {
  return v + __int_as_float(__builtin_amdgcn_update_dpp(0, __float_as_int(v), CTRL, 0xf, 0xf, true));
}
DEV float swap16_add(float v) {
  auto r = __builtin_amdgcn_permlane16_swap(__float_as_uint(v), __float_as_uint(v), false, false);
  return __uint_as_float(r[0]) + __uint_as_float(r[1]);
}
DEV float swap16_fold(float a, float b) {
  auto r = __builtin_amdgcn_permlane16_swap(__float_as_uint(a), __float_as_uint(b), false, false);
  return __uint_as_float(r[0]) + __uint_as_float(r[1]);
}
DEV float swap32_fold(float a, float b) {
  auto r = __builtin_amdgcn_permlane32_swap(__float_as_uint(a), __float_as_uint(b), false, false);
  return __uint_as_float(r[0]) + __uint_as_float(r[1]);
}
template <int CTRL> DEV float dpp_mov(float v) {
  return __int_as_float(__builtin_amdgcn_update_dpp(0, __float_as_int(v), CTRL, 0xf, 0xf, true));
}
DEV float fold8(const float (&v)[8], int lane) {
  float a[4], b[2];
#pragma unroll
  for (int k = 0; k < 4; ++k) a[k] = swap32_fold(v[k], v[k + 4]);
#pragma unroll
  for (int k = 0; k < 2; ++k) b[k] = swap16_fold(a[k], a[k + 2]);
  const bool b3 = lane & 8;
  const float c = (b3 ? b[1] : b[0]) + dpp_mov<0x128>(b3 ? b[0] : b[1]);
  return c + dpp_mov<0x1B>(dpp_mov<0x141>(c));
}
typedef __attribute__((address_space(1))) unsigned long long* gu64_ptr;
DEV void put_granule(char* p, float v, unsigned tag) {
  __hip_atomic_store((gu64_ptr)p, ((unsigned long long)tag << 32) | __float_as_uint(v), __ATOMIC_RELAXED, __HIP_MEMORY_SCOPE_AGENT);
}
DEV void vadd(float& a, float b) { asm("v_add_f32 %0, %0, %1" : "+v"(a) : "v"(b)); }
DEV float bf_lo(unsigned u) { return __uint_as_float(u << 16); }
DEV float bf_hi(unsigned u) { return __uint_as_float(u & 0xffff0000u); }

DEV void g1_loader(lds_ptr smem, const char* x, const char* wqkv, const char* wg, const char* wu, const char* wd, const char* wo, char* ws, int pi, int cj, int j,
                   int lane) {
  const bool ty = cj >= 14;
  __builtin_amdgcn_s_setprio(LPRIO);
  const int row0 = pi * 128 + cj * 8 + 4 * (j & 1);
  const char* wb[5];
  uint32_t wv[5], wl[5];
#pragma unroll
  for (int q = 0; q < 4; ++q) {
    if (ty) {
      const int i = 4 * j + q, row = 2 * i + (lane >> 5);
      wb[q] = wqkv + 2048 + 512 * (cj - 14);
      wv[q] = row * 3072 + (((lane & 31) ^ (4 * (row & 3))) << 4);
      wl[q] = i * 1024;
    } else {
      const int u = q >> 1, i = 2 * j + (q & 1), row = 4 * i + (lane >> 4);
      wb[q] = (u ? wu : wg) + cj * 256;
      wv[q] = row * 3584 + (((lane & 15) ^ (4 * (row & 3) + 2 * u)) << 4);
      wl[q] = u * 8192 + i * 1024;
    }
  }
  {
    const int row = 8 * j + (lane >> 3);
    wb[4] = wqkv + (cj < 8 ? 128 * cj : 1024 + 128 * (cj - 8));
    wv[4] = row * 3072 + (((lane & 7) ^ (4 * ((row >> 1) & 1))) << 4);
    wl[4] = 16384 + j * 1024;
  }
  const uint32_t wstep = 32 * (ty ? 3072 : 3584);
  const char* xb = x + (size_t)pi * 128 * 1024;
  uint32_t xv[4];
#pragma unroll
  for (int q = 0; q < 4; ++q) {
    const int t = 8 * (4 * j + q) + (lane >> 3);
    xv[q] = t * 1024 + (((lane & 7) ^ ((t >> 1) & 7)) << 4);
  }
  const uint32_t lbase = lds_addr(smem);
  const int koff = g1_koff(pi, cj);
  int wslot = 0;
  auto issue_w = [&](int t) {
    const int s = (t + 2 * koff) & 15;
    const uint32_t so = lbase + G1_W + wslot * G1_WS;
    wslot = wslot == G1_WN - 1 ? 0 : wslot + 1;
#pragma unroll
    for (int q = 0; q < 4; ++q) glds16(wv[q], wb[q] + (size_t)wstep * s, so + wl[q]);
    glds16(wv[4], wb[4] + (size_t)(32 * 3072) * s, so + wl[4]);
  };
  auto issue_xh = [&](int m, int h) {
    const uint32_t so = lbase + G1_X + (m % G1_XN) * G1_XS + j * 4096;
#pragma unroll
    for (int q = 2 * h; q < 2 * h + 2; ++q) glds16(xv[q], xb + ((m + koff) & 7) * 128, so + q * 1024);
  };
  auto issue_x = [&](int m) { issue_xh(m, 0); issue_xh(m, 1); };
  issue_w(0); issue_x(0); issue_w(1);
  if (PRON == 23) { issue_w(2); issue_x(1); }
  const int p = pi & 7, blk = j == 0 ? p : (p < 2 ? p + 8 : 0);
  const bool cs_on = j == 0 || (j == 1 && p < 2);
  int cso, csk, csr;
  {
    const int ch = blk / 5, b = blk % 5, hi = lane >> 5, g16 = (lane >> 4) & 1, q4 = (lane >> 2) & 3, p4 = lane & 3;
    if (b == 4) {
      const int c = 4 * ch + 2 * g16 + (p4 >> 1);
      cso = 16384 + (8 * hi + q4) * 128 + ((c ^ (4 * (q4 >> 1))) << 4) + 8 * (p4 & 1);
      csk = 2048; csr = 512;
    } else if (ty) {
      const int c = 4 * (4 * ch + b) + 2 * g16 + (p4 >> 1);
      cso = (8 * hi + q4) * 512 + ((c ^ (4 * q4)) << 4) + 8 * (p4 & 1);
      csk = 8192; csr = 2048;
    } else {
      const int u = p4 >> 1, c = 2 * (4 * ch + b) + g16;
      cso = u * 8192 + (8 * hi + q4) * 256 + ((c ^ (4 * q4 + 2 * u)) << 4) + 8 * (p4 & 1);
      csk = 4096; csr = 1024;
    }
  }
  const unsigned selw = ((lane & 15) == 0 && (lane & 16) == 0) || ((lane & 15) == 4 && (lane & 16) != 0) ? 0x3F803F80u : 0u;
  const bf16x8 sel = as_frag(u32x4{selw, selw, selw, selw});
  f32x4 cs = {0.f, 0.f, 0.f, 0.f};
  auto cs_add = [&](int slot) {
    lds_ptr a = smem + G1_W + slot * G1_WS + cso;
    const bf16x8 f0 = lds_tr8(a, a + csr), f1 = lds_tr8(a + csk, a + csk + csr);
    cs = __builtin_amdgcn_mfma_f32_16x16x32_bf16(sel, f0, cs, 0, 0, 0);
    cs = __builtin_amdgcn_mfma_f32_16x16x32_bf16(sel, f1, cs, 0, 0, 0);
  };
  if (PRON == 23) wait_vm<14>();
  else wait_vm<5>();
  bar();
  if (PRON != 23) { issue_w(2); issue_x(1); }
  issue_xh(2, 0);
  issue_w(3);
  uint32_t ep;
  u32x4 r0, r1, r2, r3;
  asm volatile("global_load_dword %0, %5, %6 sc1\n\ts_cmp_lt_u32 %9, 2\n\ts_cbranch_scc1 Lskip_%=\n\t"
               "global_load_dwordx4 %1, %7, %8\n\tglobal_load_dwordx4 %2, %7, %8 offset:1024\n\t"
               "global_load_dwordx4 %3, %7, %8 offset:2048\n\tglobal_load_dwordx4 %4, %7, %8 offset:3072\n\tLskip_%=:"
               : "={v64}"(ep), "={v[48:51]}"(r0), "={v[52:55]}"(r1), "={v[56:59]}"(r2), "={v[60:63]}"(r3)
               : "v"(0), "s"(ws + WS_EP), "v"(lane * 16), "s"(x + (size_t)row0 * 1024), "s"(j) : "memory", "scc");
#define WAIT_EARLY(NA, NB)                                                                                                       \
  asm volatile("s_cmp_lt_u32 %0, 2\n\ts_cbranch_scc1 La_%=\n\ts_waitcnt vmcnt(" #NB ")\n\ts_branch Lb_%=\n\tLa_%=:\n\t"          \
               "s_waitcnt vmcnt(" #NA ")\n\tLb_%=:" ::"s"(j) : "memory", "scc")
  WAIT_EARLY(17, 21);
  bar();
  issue_xh(2, 1);
  issue_w(4);
  WAIT_EARLY(15, 19);
  bar();
  issue_xh(3, 0);
  issue_w(5);
  asm volatile("s_waitcnt vmcnt(14)" : "+{v64}"(ep), "+{v[48:51]}"(r0), "+{v[52:55]}"(r1), "+{v[56:59]}"(r2), "+{v[60:63]}"(r3)::"memory");
  const unsigned tag = (unsigned)uniform((int)ep) + 1u;
  u32x4 ga, gb;
  const char* gcs = ws + WS_CSG + (size_t)cj * 320 * 16;
  const char* grs = ws + WS_RSG + (size_t)pi * 128 * 16;
  const char* gsrc2 = j == 0 ? gcs + 256 * 16 : grs + (j == 1 ? 0 : 64 * 16);
  auto gather = [&]() {
    asm volatile("global_load_dwordx4 %0, %2, %3 sc1\n\tglobal_load_dwordx4 %1, %2, %4 sc1"
                 : "=&{v[48:51]}"(ga), "=&{v[52:55]}"(gb) : "v"(lane * 16), "s"(gcs + j * 64 * 16), "s"(gsrc2) : "memory");
  };
  float st[8], stc = 0.f;
#pragma unroll
  for (int m = 0; m < 6; ++m) {
    if (m > 0) {
      const bool xa = j >= 2 ? (m == 3 || m == 4) : (m == 4 || m == 5);
      const bool xb = j >= 2 ? m == 3 : m == 4;
      if (xa) wait_vm<15>();
      else wait_vm<14>();
      bar();
      issue_xh(m + 2, 1);
      issue_w(2 * m + 4);
      if (xb) wait_vm<15>();
      else wait_vm<14>();
      bar();
      if (m < 5) issue_xh(m + 3, 0);
      issue_w(2 * m + 5);
    }
    if (j >= 2) {
      const unsigned ones = 0x3F803F80u;
      if (m == 0) {
        const u32x4 rr[4] = {r0, r1, r2, r3};
#pragma unroll
        for (int i = 0; i < 8; ++i) st[i] = 0.f;
#pragma unroll
        for (int e = 0; e < 4; ++e)
#pragma unroll
          for (int i = 0; i < 4; ++i) {
            st[2 * i] = dot2(rr[i][e], ones, st[2 * i]);
            st[2 * i + 1] = dot2(rr[i][e], rr[i][e], st[2 * i + 1]);
          }
      } else if (m == 1) {
        stc = fold8(st, lane);
        stc += dpp_mov<0x4E>(stc);
        stc += dpp_mov<0xB1>(stc);
      } else if (m == 2) {
        const float o = dpp_mov<0x128>(stc);
        const bool b3 = lane & 8;
        const float mu = (b3 ? o : stc) * (1.0f / DM);
        const float var = fmaxf((b3 ? stc : o) * (1.0f / DM) - mu * mu, 0.f);
        const float rs = __builtin_amdgcn_rsqf(var + 1e-5f);
        if ((lane & 7) == 0) put_granule(ws + WS_RSG + (size_t)(row0 + (lane >> 4)) * 16 + (lane & 8), b3 ? rs * mu : rs, tag);
      }
    } else if (m < 4) {
      cs_add((2 * m + 1) % G1_WN);
      cs_add((2 * m + 2) % G1_WN);
      if (m == 3) {
        char* dst = cs_on ? ws + WS_CSG + ((size_t)(cj * 320 + blk * 32 + lane) * 2 + (pi >> 3)) * 8 : ws + WS_DUMMY + blockIdx.x * 512 + lane * 8;
        if (lane < 32) put_granule(dst, cs[0], tag);
      }
    }
  }
  wait_vm<12>();
  bar();
  gather();
  wait_vm<7>();
  bar();
  wait_vm<2>();
  bar();
  asm volatile("s_waitcnt vmcnt(0)" : "+{v[48:51]}"(ga), "+{v[52:55]}"(gb)::"memory");
  for (int spin = 0; spin < (1 << 18); ++spin) {
    if (__builtin_amdgcn_ballot_w64(ga[1] != tag || ga[3] != tag || gb[1] != tag || gb[3] != tag) == 0) break;
    __builtin_amdgcn_s_sleep(2);
    gather();
    asm volatile("s_waitcnt vmcnt(0)" : "+{v[48:51]}"(ga), "+{v[52:55]}"(gb)::"memory");
  }
  st16w(ws + WS_O + (size_t)blockIdx.x * 8192 + j * 2048 + lane * 16, u32x4{~0u, ~0u, ~0u, ~0u});
  st16w(ws + WS_O + (size_t)blockIdx.x * 8192 + j * 2048 + 1024 + lane * 16, u32x4{~0u, ~0u, ~0u, ~0u});
  ((lds_f32_ptr)(smem + G1_CS))[64 * j + lane] = __uint_as_float(ga[0]) + __uint_as_float(ga[2]);
  if (j == 0) ((lds_f32_ptr)(smem + G1_CS))[256 + lane] = __uint_as_float(gb[0]) + __uint_as_float(gb[2]);
  else if (j < 3) {
    ((lds_f32_ptr)(smem + G1_RS))[64 * (j - 1) + lane] = __uint_as_float(gb[0]);
    ((lds_f32_ptr)(smem + G1_RM))[64 * (j - 1) + lane] = __uint_as_float(gb[2]);
  }
  bar();
  if (blockIdx.x == 0 && j == 3) {
    for (int spin = 0; spin < (1 << 18); ++spin) {
      bool ok = true;
#pragma unroll
      for (int i = 0; i < 4; ++i) {
        const int wgi = 4 * lane + i;
        const unsigned long long g = __hip_atomic_load((gu64_ptr)(ws + WS_RSG + (size_t)((wgi >> 4) * 128 + (wgi & 15) * 8) * 16),
                                                       __ATOMIC_RELAXED, __HIP_MEMORY_SCOPE_AGENT);
        ok = ok && (unsigned)(g >> 32) == tag;
      }
      if (__builtin_amdgcn_ballot_w64(!ok) == 0) break;
      __builtin_amdgcn_s_sleep(8);
    }
    if (lane == 0) __hip_atomic_store((__attribute__((address_space(1))) unsigned*)(ws + WS_EP), tag, __ATOMIC_RELAXED, __HIP_MEMORY_SCOPE_AGENT);
  }
}

template <bool Y>
DEV void g1_compute(lds_ptr smem, const char* x, const char* wqkv, const char* wg, const char* wu, char* ws, int pi, int cj, int w,
                    int lane) {
  const int tb = w & 3, ch = w >> 2, hi = lane >> 5, l31 = lane & 31;
  const int g16 = (lane >> 4) & 1, q4 = (lane >> 2) & 3, p4 = lane & 3;
  int wo[5];
#pragma unroll
  for (int b = 0; b < 4; ++b) {
    if (Y) {
      const int c = 4 * (4 * ch + b) + 2 * g16 + (p4 >> 1);
      wo[b] = (8 * hi + q4) * 512 + ((c ^ (4 * q4)) << 4) + 8 * (p4 & 1);
    } else {
      const int u = p4 >> 1, c = 2 * (4 * ch + b) + g16;
      wo[b] = u * 8192 + (8 * hi + q4) * 256 + ((c ^ (4 * q4 + 2 * u)) << 4) + 8 * (p4 & 1);
    }
  }
  {
    const int c = 4 * ch + 2 * g16 + (p4 >> 1);
    wo[4] = 16384 + (8 * hi + q4) * 128 + ((c ^ (4 * (q4 >> 1))) << 4) + 8 * (p4 & 1);
  }
  const int tl = 32 * tb + l31;
  const int xo = tl * 128 + ((hi ^ ((tl >> 1) & 7)) << 4);
  f32x16 acc0, acc1, acc2, acc3, acc4;
#pragma unroll
  for (int r = 0; r < 16; ++r) acc0[r] = acc1[r] = acc2[r] = acc3[r] = acc4[r] = 0.f;
  const unsigned ones = 0x3F803F80u;
  const uint32_t lbase = lds_addr(smem);
  const uint32_t aw = lbase + G1_W, ab = aw + 3 * G1_WS, ax = lbase + G1_X + xo;
#define G1_SEG(TEXT)                                                                                                             \
  asm volatile(TEXT                                                                                                              \
               : "+{v[0:15]}"(acc0), "+{v[16:31]}"(acc1), "+{v[32:47]}"(acc2), "+{v[48:63]}"(acc3), "+{v[64:79]}"(acc4)          \
               : "{v128}"(aw + wo[0]), "{v129}"(aw + wo[1]), "{v130}"(aw + wo[2]), "{v131}"(aw + wo[3]), "{v132}"(aw + wo[4]),   \
                 "{v133}"(ab + wo[0]), "{v134}"(ab + wo[1]), "{v135}"(ab + wo[2]), "{v136}"(ab + wo[3]), "{v137}"(ab + wo[4]),   \
                 "{v138}"(ax), "{v139}"(ax ^ 32), "{v140}"(ax ^ 64), "{v141}"(ax ^ 96)                                            \
               : "memory", "v80", "v81", "v82", "v83", "v84", "v85", "v86", "v87", "v88", "v89", "v90", "v91", "v92", "v93",     \
                 "v94", "v95", "v96", "v97", "v98", "v99", "v100", "v101", "v102", "v103", "v104", "v105", "v106", "v107",       \
                 "v108", "v109", "v110", "v111", "v112", "v113", "v114", "v115", "v116", "v117", "v118", "v119", "v120",         \
                 "v121", "v122", "v123", "v124", "v125", "v126", "v127")
  if (Y) G1_SEG(G1_LOOP_Y);
  else G1_SEG(G1_LOOP_X);
  bar();
  const f32x16 acc[5] = {acc0, acc1, acc2, acc3, acc4};
  const int tbg = pi * 4 + tb;
  const float rs = ((lds_f32_ptr)(smem + G1_RS))[tb * 32 + l31];
  const float rm = ((lds_f32_ptr)(smem + G1_RM))[tb * 32 + l31];
  auto fold_t = [&](const f32x16& a, int blk, float srs, float srm) {
    f32x16 o;
#pragma unroll
    for (int g = 0; g < 4; ++g) {
      const f32x4 c = ldsf4(smem + G1_CS + (blk * 32 + 8 * g + 4 * hi) * 4);
#pragma unroll
      for (int e = 0; e < 4; e += 2) {
        const f32x2 r = __builtin_elementwise_fma(f32x2{a[4 * g + e], a[4 * g + e + 1]}, splat2(srs), f32x2{c[e], c[e + 1]} * splat2(-srm));
        o[4 * g + e] = r[0];
        o[4 * g + e + 1] = r[1];
      }
    }
    return o;
  };
  {
    const bool isq = cj < 8;
    const float qs = isq ? 0.25f * 1.4426950408889634f : 1.0f;
    const f32x16 v = fold_t(acc[4], ch * 5 + 4, rs * qs, rm * qs);
    const int head = 4 * (isq ? cj : cj - 8) + 2 * ch;
    char* dst = ws + (isq ? WS_Q : WS_K) + ((size_t)head * NTB + tbg) * 1024 + lane * 16;
    ST1(dst, pack8(v[0], v[1], v[2], v[3], v[4], v[5], v[6], v[7]));
    ST1(dst + NTB * 1024, pack8(v[8], v[9], v[10], v[11], v[12], v[13], v[14], v[15]));
  }
  if (Y) {
    f32x4 ra[4], rmm[4];
#pragma unroll
    for (int g = 0; g < 4; ++g) {
      ra[g] = ldsf4(smem + G1_RS + (tb * 32 + 8 * g + 4 * hi) * 4);
      rmm[g] = ldsf4(smem + G1_RM + (tb * 32 + 8 * g + 4 * hi) * 4);
    }
#pragma unroll
    for (int b = 0; b < 4; ++b) {
      const float c = ((lds_f32_ptr)(smem + G1_CS))[(ch * 5 + b) * 32 + l31];
      f32x16 v;
#pragma unroll
      for (int g = 0; g < 4; ++g)
#pragma unroll
        for (int e = 0; e < 4; ++e) v[4 * g + e] = __builtin_fmaf(acc[b][4 * g + e], ra[g][e], -rmm[g][e] * c);
      const int head = 16 * (cj - 14) + 2 * (4 * ch + b) + (l31 >> 4);
      char* dst = ws + WS_V + ((size_t)head * NTB + tbg) * VT + hi * 272;
      const int dd = l31 & 15;
      ST1(dst + 16 + 16 * dd, pack8(v[0], v[1], v[2], v[3], v[4], v[5], v[6], v[7]));
      ST1(dst + 544 + 16 + 16 * dd, pack8(v[8], v[9], v[10], v[11], v[12], v[13], v[14], v[15]));
      if (dd == 0) {
        const u32x4 o4 = {ones, ones, ones, ones};
        ST1(dst, o4);
        ST1(dst + 544, o4);
      }
    }
  } else {
#pragma unroll
    for (int b = 0; b < 4; ++b) {
      const f32x16 v = fold_t(acc[b], ch * 5 + b, rs, rm);
      float h[8];
#pragma unroll
      for (int e = 0; e < 4; e += 2) {
        const f32x2 h0 = gelu_mul2(f32x2{v[e], v[e + 1]}, f32x2{v[4 + e], v[5 + e]});
        const f32x2 h1 = gelu_mul2(f32x2{v[8 + e], v[9 + e]}, f32x2{v[12 + e], v[13 + e]});
        h[e] = h0[0]; h[e + 1] = h0[1];
        h[4 + e] = h1[0]; h[5 + e] = h1[1];
      }
      ST1(ws + WS_H + ((size_t)tbg * NKG + 8 * cj + 4 * ch + b) * 1024 + lane * 16,
          pack8(h[0], h[1], h[2], h[3], h[4], h[5], h[6], h[7]));
    }
  }
}

__global__ __launch_bounds__(768, 3) void k_gemm1(const char* x, const char* wqkv, const char* wg, const char* wu, const char* wd, const char* wo, char* ws) {
  __shared__ __attribute__((aligned(16))) char smem_raw[G1_LDS + 256];
  lds_ptr smem = (lds_ptr)smem_raw;
  const int lane = threadIdx.x & 63, w = uniform(threadIdx.x >> 6);
  const int xcd = blockIdx.x & 7, loc = blockIdx.x >> 3;
  const int pi = (xcd & 1) * 8 + (loc & 7), cj = (xcd >> 1) * 4 + (loc >> 3);
  if (w >= 8) {
    g1_loader(smem, x, wqkv, wg, wu, wd, wo, ws, pi, cj, w - 8, lane);
  } else if (cj >= 14) g1_compute<true>(smem, x, wqkv, wg, wu, ws, pi, cj, w, lane);
  else g1_compute<false>(smem, x, wqkv, wg, wu, ws, pi, cj, w, lane);
}

#define AT_PRO_ASM \
  "global_load_dwordx4 v[80:83], %[qo], %[qb]\n\t" \
  "v_mov_b64 v[16:17], 0\n\t" \
  "v_mov_b64 v[18:19], 0\n\t" \
  "v_mov_b64 v[20:21], 0\n\t" \
  "v_mov_b64 v[22:23], 0\n\t" \
  "v_mov_b64 v[24:25], 0\n\t" \
  "v_mov_b64 v[26:27], 0\n\t" \
  "v_mov_b64 v[28:29], 0\n\t" \
  "v_mov_b64 v[30:31], 0\n\t" \
  "v_mov_b64 v[32:33], 0\n\t" \
  "v_mov_b64 v[34:35], 0\n\t" \
  "v_mov_b64 v[36:37], 0\n\t" \
  "v_mov_b64 v[38:39], 0\n\t" \
  "v_mov_b64 v[40:41], 0\n\t" \
  "v_mov_b64 v[42:43], 0\n\t" \
  "v_mov_b64 v[44:45], 0\n\t" \
  "v_mov_b64 v[46:47], 0\n\t" \
  "v_mov_b64 v[92:93], 0\n\t" \
  "v_mov_b64 v[94:95], 0\n\t" \
  "v_mov_b64 v[96:97], 0\n\t" \
  "v_mov_b64 v[98:99], 0\n\t" \
  "v_mov_b64 v[100:101], 0\n\t" \
  "v_mov_b64 v[102:103], 0\n\t" \
  "v_mov_b64 v[104:105], 0\n\t" \
  "v_mov_b64 v[106:107], 0\n\t" \
  "s_waitcnt vmcnt(0) lgkmcnt(0)\n\t" \
  "s_barrier\n\t" \
  "ds_read_b128 v[84:87], %[ka]\n\t" \
  "ds_read_b128 v[88:91], %[ka] offset:1024\n\t" \
  "s_mov_b64 exec, %[em]\n\t" \
  "ds_read_b128 v[92:95], %[va]\n\t" \
  "ds_read_b128 v[96:99], %[va] offset:544\n\t" \
  "s_mov_b64 exec, -1\n\t" \
  "s_waitcnt lgkmcnt(3)\n\t" \
  "v_mfma_f32_32x32x16_bf16 v[48:63], v[84:87], v[80:83], 0\n\t" \
  "s_waitcnt lgkmcnt(2)\n\t" \
  "v_mfma_f32_32x32x16_bf16 v[64:79], v[88:91], v[80:83], 0\n\t" \
  "s_nop 11\n\t" \
  "v_max3_f32 v2, v48, v49, v50\n\t" \
  "v_max3_f32 v3, v51, v52, v53\n\t" \
  "v_max3_f32 v4, v54, v55, v56\n\t" \
  "v_max3_f32 v5, v57, v58, v59\n\t" \
  "v_max3_f32 v6, v60, v61, v62\n\t" \
  "v_max3_f32 v7, v63, v64, v65\n\t" \
  "v_max3_f32 v8, v66, v67, v68\n\t" \
  "v_max3_f32 v9, v69, v70, v71\n\t" \
  "v_max3_f32 v10, v72, v73, v74\n\t" \
  "v_max3_f32 v11, v75, v76, v77\n\t" \
  "v_max_f32 v12, v78, v79\n\t" \
  "v_mov_b64 v[72:73], 0\n\t" \
  "v_mov_b64 v[74:75], 0\n\t" \
  "v_mov_b64 v[76:77], 0\n\t" \
  "v_mov_b64 v[78:79], 0\n\t" \
  "v_max3_f32 v2, v2, v3, v4\n\t" \
  "v_max3_f32 v5, v5, v6, v7\n\t" \
  "v_max3_f32 v8, v8, v9, v10\n\t" \
  "v_max_f32 v11, v11, v12\n\t" \
  "s_nop 0\n\t" \
  "v_max3_f32 v2, v2, v5, v8\n\t" \
  "s_nop 0\n\t" \
  "v_max_f32 v0, v2, v11\n\t" \
  "s_nop 0\n\t" \
  "v_mov_b32 v1, v0\n\t" \
  "s_nop 1\n\t" \
  "v_permlane32_swap_b32 v0, v1\n\t" \
  "s_nop 1\n\t" \
  "v_max_f32 v0, v0, v1\n\t" \
  "s_nop 0\n\t" \
  "v_sub_f32 v0, 0xc2b00000, v0\n\t" \
  "s_nop 0\n\t" \
  "v_mov_b32 v1, v0\n\t" \
  "s_nop 0\n\t" \
  "v_pk_add_f32 v[48:49], v[48:49], v[0:1]\n\t" \
  "v_pk_add_f32 v[50:51], v[50:51], v[0:1]\n\t" \
  "v_pk_add_f32 v[52:53], v[52:53], v[0:1]\n\t" \
  "v_pk_add_f32 v[54:55], v[54:55], v[0:1]\n\t" \
  "v_pk_add_f32 v[56:57], v[56:57], v[0:1]\n\t" \
  "v_pk_add_f32 v[58:59], v[58:59], v[0:1]\n\t" \
  "v_pk_add_f32 v[60:61], v[60:61], v[0:1]\n\t" \
  "v_pk_add_f32 v[62:63], v[62:63], v[0:1]\n\t" \
  "v_mov_b64 v[2:3], v[0:1]\n\t" \
  "v_mov_b64 v[4:5], v[0:1]\n\t" \
  "v_mov_b64 v[6:7], v[0:1]\n\t" \
  "v_mov_b64 v[8:9], v[0:1]\n\t" \
  "v_mov_b64 v[10:11], v[0:1]\n\t" \
  "v_mov_b64 v[12:13], v[0:1]\n\t" \
  "v_mov_b64 v[14:15], v[0:1]\n\t" \
  "s_nop 1\n\t"
#define AT_P5_ASM \
  "ds_read_b128 v[32:35], %[pp] offset:0\n\t" \
  "ds_read_b128 v[36:39], %[pp] offset:16384\n\t" \
  "ds_read_b128 v[40:43], %[pp] offset:32768\n\t" \
  "ds_read_b128 v[44:47], %[pp] offset:49152\n\t" \
  "ds_read_b128 v[48:51], %[pp] offset:1024\n\t" \
  "ds_read_b128 v[52:55], %[pp] offset:17408\n\t" \
  "ds_read_b128 v[56:59], %[pp] offset:33792\n\t" \
  "ds_read_b128 v[60:63], %[pp] offset:50176\n\t" \
  "ds_read_b64 v[146:147], %[xa]\n\t" \
  "v_xor_b32 v144, 16, %[xa]\n\t" \
  "ds_read_b64 v[148:149], v144\n\t" \
  "s_waitcnt lgkmcnt(6)\n\t" \
  "v_pk_add_f32 v[32:33], v[32:33], v[36:37]\n\t" \
  "v_pk_add_f32 v[40:41], v[40:41], v[44:45]\n\t" \
  "v_pk_add_f32 v[34:35], v[34:35], v[38:39]\n\t" \
  "v_pk_add_f32 v[42:43], v[42:43], v[46:47]\n\t" \
  "s_nop 0\n\t" \
  "v_pk_add_f32 v[136:137], v[32:33], v[40:41]\n\t" \
  "v_pk_add_f32 v[138:139], v[34:35], v[42:43]\n\t" \
  "s_waitcnt lgkmcnt(2)\n\t" \
  "v_pk_add_f32 v[48:49], v[48:49], v[52:53]\n\t" \
  "v_pk_add_f32 v[56:57], v[56:57], v[60:61]\n\t" \
  "v_pk_add_f32 v[50:51], v[50:51], v[54:55]\n\t" \
  "v_pk_add_f32 v[58:59], v[58:59], v[62:63]\n\t" \
  "s_nop 0\n\t" \
  "v_pk_add_f32 v[140:141], v[48:49], v[56:57]\n\t" \
  "v_pk_add_f32 v[142:143], v[50:51], v[58:59]\n\t" \
  "s_barrier\n\t" \
  "v_add_u32 v144, %[dl], %[r0]\n\t" \
  "v_add_u32 v145, %[dl], %[r1]\n\t" \
  "global_load_dwordx4 v[0:3], %[o0], %[ob] offset:0 sc1\n\t" \
  "ds_read_b64_tr_b16 v[32:33], %[r0] offset:0\n\t" \
  "ds_read_b64_tr_b16 v[34:35], %[r0] offset:1024\n\t" \
  "ds_read_b64_tr_b16 v[36:37], %[r1] offset:0\n\t" \
  "ds_read_b64_tr_b16 v[38:39], %[r1] offset:1024\n\t" \
  "global_load_dwordx4 v[4:7], %[o0], %[ob] offset:1024 sc1\n\t" \
  "ds_read_b64_tr_b16 v[40:41], %[r0] offset:2048\n\t" \
  "ds_read_b64_tr_b16 v[42:43], %[r0] offset:3072\n\t" \
  "ds_read_b64_tr_b16 v[44:45], %[r1] offset:2048\n\t" \
  "ds_read_b64_tr_b16 v[46:47], %[r1] offset:3072\n\t" \
  "global_load_dwordx4 v[8:11], %[o0], %[ob] offset:2048 sc1\n\t" \
  "ds_read_b64_tr_b16 v[48:49], %[r0] offset:4096\n\t" \
  "ds_read_b64_tr_b16 v[50:51], %[r0] offset:5120\n\t" \
  "ds_read_b64_tr_b16 v[52:53], %[r1] offset:4096\n\t" \
  "ds_read_b64_tr_b16 v[54:55], %[r1] offset:5120\n\t" \
  "global_load_dwordx4 v[12:15], %[o0], %[ob] offset:3072 sc1\n\t" \
  "ds_read_b64_tr_b16 v[56:57], %[r0] offset:6144\n\t" \
  "ds_read_b64_tr_b16 v[58:59], %[r0] offset:7168\n\t" \
  "ds_read_b64_tr_b16 v[60:61], %[r1] offset:6144\n\t" \
  "ds_read_b64_tr_b16 v[62:63], %[r1] offset:7168\n\t" \
  "global_load_dwordx4 v[16:19], %[o1], %[ob] offset:0 sc1\n\t" \
  "ds_read_b64_tr_b16 v[64:65], %[r0] offset:8192\n\t" \
  "ds_read_b64_tr_b16 v[66:67], %[r0] offset:9216\n\t" \
  "ds_read_b64_tr_b16 v[68:69], %[r1] offset:8192\n\t" \
  "ds_read_b64_tr_b16 v[70:71], %[r1] offset:9216\n\t" \
  "global_load_dwordx4 v[20:23], %[o1], %[ob] offset:1024 sc1\n\t" \
  "ds_read_b64_tr_b16 v[72:73], %[r0] offset:10240\n\t" \
  "ds_read_b64_tr_b16 v[74:75], %[r0] offset:11264\n\t" \
  "ds_read_b64_tr_b16 v[76:77], %[r1] offset:10240\n\t" \
  "ds_read_b64_tr_b16 v[78:79], %[r1] offset:11264\n\t" \
  "global_load_dwordx4 v[24:27], %[o1], %[ob] offset:2048 sc1\n\t" \
  "ds_read_b64_tr_b16 v[80:81], %[r0] offset:12288\n\t" \
  "ds_read_b64_tr_b16 v[82:83], %[r0] offset:13312\n\t" \
  "ds_read_b64_tr_b16 v[84:85], %[r1] offset:12288\n\t" \
  "ds_read_b64_tr_b16 v[86:87], %[r1] offset:13312\n\t" \
  "global_load_dwordx4 v[28:31], %[o1], %[ob] offset:3072 sc1\n\t" \
  "ds_read_b64_tr_b16 v[88:89], %[r0] offset:14336\n\t" \
  "ds_read_b64_tr_b16 v[90:91], v144 offset:15360\n\t" \
  "ds_read_b64_tr_b16 v[92:93], %[r1] offset:14336\n\t" \
  "ds_read_b64_tr_b16 v[94:95], v145 offset:15360\n\t" \
  "v_mul_f32 v128, 0xbfb8aa3b, v136\n\t" \
  "v_mul_f32 v129, 0xbfb8aa3b, v137\n\t" \
  "v_mul_f32 v130, 0xbfb8aa3b, v138\n\t" \
  "v_mul_f32 v131, 0xbfb8aa3b, v139\n\t" \
  "v_mul_f32 v132, 0xbfb8aa3b, v140\n\t" \
  "v_mul_f32 v133, 0xbfb8aa3b, v141\n\t" \
  "v_mul_f32 v134, 0xbfb8aa3b, v142\n\t" \
  "v_mul_f32 v135, 0xbfb8aa3b, v143\n\t" \
  "v_exp_f32 v128, v128\n\t" \
  "v_exp_f32 v129, v129\n\t" \
  "v_exp_f32 v130, v130\n\t" \
  "v_exp_f32 v131, v131\n\t" \
  "v_exp_f32 v132, v132\n\t" \
  "v_exp_f32 v133, v133\n\t" \
  "v_exp_f32 v134, v134\n\t" \
  "v_exp_f32 v135, v135\n\t" \
  "v_add_f32 v128, 1.0, v128\n\t" \
  "v_add_f32 v129, 1.0, v129\n\t" \
  "v_add_f32 v130, 1.0, v130\n\t" \
  "v_add_f32 v131, 1.0, v131\n\t" \
  "v_add_f32 v132, 1.0, v132\n\t" \
  "v_add_f32 v133, 1.0, v133\n\t" \
  "v_add_f32 v134, 1.0, v134\n\t" \
  "v_add_f32 v135, 1.0, v135\n\t" \
  "v_rcp_f32 v128, v128\n\t" \
  "v_rcp_f32 v129, v129\n\t" \
  "v_rcp_f32 v130, v130\n\t" \
  "v_rcp_f32 v131, v131\n\t" \
  "v_rcp_f32 v132, v132\n\t" \
  "v_rcp_f32 v133, v133\n\t" \
  "v_rcp_f32 v134, v134\n\t" \
  "v_rcp_f32 v135, v135\n\t" \
  "v_mul_f32 v128, v136, v128\n\t" \
  "v_mul_f32 v129, v137, v129\n\t" \
  "v_mul_f32 v130, v138, v130\n\t" \
  "v_mul_f32 v131, v139, v131\n\t" \
  "v_mul_f32 v132, v140, v132\n\t" \
  "v_mul_f32 v133, v141, v133\n\t" \
  "v_mul_f32 v134, v142, v134\n\t" \
  "v_mul_f32 v135, v143, v135\n\t" \
  "s_mov_b32 %[cnt], 0x40000\n\t" \
  "s_waitcnt vmcnt(7) lgkmcnt(15)\n\t" \
  "v_mfma_f32_32x32x16_bf16 v[96:111], v[32:35], v[0:3], 0\n\t" \
  "v_mfma_f32_32x32x16_bf16 v[112:127], v[36:39], v[0:3], 0\n\t" \
  "v_max3_u32 v136, v0, v1, v2\n\t" \
  "v_max_u32 v136, v136, v3\n\t" \
  "s_waitcnt vmcnt(6) lgkmcnt(15)\n\t" \
  "v_mfma_f32_32x32x16_bf16 v[96:111], v[40:43], v[4:7], v[96:111]\n\t" \
  "v_mfma_f32_32x32x16_bf16 v[112:127], v[44:47], v[4:7], v[112:127]\n\t" \
  "v_max3_u32 v137, v4, v5, v6\n\t" \
  "v_max_u32 v137, v137, v7\n\t" \
  "s_waitcnt vmcnt(5) lgkmcnt(15)\n\t" \
  "v_mfma_f32_32x32x16_bf16 v[96:111], v[48:51], v[8:11], v[96:111]\n\t" \
  "v_mfma_f32_32x32x16_bf16 v[112:127], v[52:55], v[8:11], v[112:127]\n\t" \
  "v_max3_u32 v138, v8, v9, v10\n\t" \
  "v_max_u32 v138, v138, v11\n\t" \
  "s_waitcnt vmcnt(4) lgkmcnt(15)\n\t" \
  "v_mfma_f32_32x32x16_bf16 v[96:111], v[56:59], v[12:15], v[96:111]\n\t" \
  "v_mfma_f32_32x32x16_bf16 v[112:127], v[60:63], v[12:15], v[112:127]\n\t" \
  "v_max3_u32 v139, v12, v13, v14\n\t" \
  "v_max_u32 v139, v139, v15\n\t" \
  "s_waitcnt vmcnt(3) lgkmcnt(12)\n\t" \
  "v_mfma_f32_32x32x16_bf16 v[96:111], v[64:67], v[16:19], v[96:111]\n\t" \
  "v_mfma_f32_32x32x16_bf16 v[112:127], v[68:71], v[16:19], v[112:127]\n\t" \
  "v_max3_u32 v140, v16, v17, v18\n\t" \
  "v_max_u32 v140, v140, v19\n\t" \
  "s_waitcnt vmcnt(2) lgkmcnt(8)\n\t" \
  "v_mfma_f32_32x32x16_bf16 v[96:111], v[72:75], v[20:23], v[96:111]\n\t" \
  "v_mfma_f32_32x32x16_bf16 v[112:127], v[76:79], v[20:23], v[112:127]\n\t" \
  "v_max3_u32 v141, v20, v21, v22\n\t" \
  "v_max_u32 v141, v141, v23\n\t" \
  "s_waitcnt vmcnt(1) lgkmcnt(4)\n\t" \
  "v_mfma_f32_32x32x16_bf16 v[96:111], v[80:83], v[24:27], v[96:111]\n\t" \
  "v_mfma_f32_32x32x16_bf16 v[112:127], v[84:87], v[24:27], v[112:127]\n\t" \
  "v_max3_u32 v142, v24, v25, v26\n\t" \
  "v_max_u32 v142, v142, v27\n\t" \
  "s_waitcnt vmcnt(0) lgkmcnt(0)\n\t" \
  "v_mfma_f32_32x32x16_bf16 v[96:111], v[88:91], v[28:31], v[96:111]\n\t" \
  "v_mfma_f32_32x32x16_bf16 v[112:127], v[92:95], v[28:31], v[112:127]\n\t" \
  "v_max3_u32 v143, v28, v29, v30\n\t" \
  "v_max_u32 v143, v143, v31\n\t" \
  "v_max3_u32 v144, v136, v137, v138\n\t" \
  "v_max3_u32 v145, v139, v140, v141\n\t" \
  "v_max3_u32 v144, v144, v142, v143\n\t" \
  "v_max_u32 v144, v144, v145\n\t" \
  "v_cmp_eq_u32 vcc, -1, v144\n\t" \
  "s_cbranch_vccz Lp5_done_%=\n\t" \
  "Lp5_poll_%=:\n\t" \
  "s_sub_u32 %[cnt], %[cnt], 1\n\t" \
  "s_cbranch_scc1 Lp5_done_%=\n\t" \
  "v_cmp_eq_u32 vcc, -1, v136\n\t" \
  "s_cbranch_vccz Lp5_s0_%=\n\t" \
  "global_load_dwordx4 v[0:3], %[o0], %[ob] offset:0 sc1\n\t" \
  "Lp5_s0_%=:\n\t" \
  "v_cmp_eq_u32 vcc, -1, v137\n\t" \
  "s_cbranch_vccz Lp5_s1_%=\n\t" \
  "global_load_dwordx4 v[4:7], %[o0], %[ob] offset:1024 sc1\n\t" \
  "Lp5_s1_%=:\n\t" \
  "v_cmp_eq_u32 vcc, -1, v138\n\t" \
  "s_cbranch_vccz Lp5_s2_%=\n\t" \
  "global_load_dwordx4 v[8:11], %[o0], %[ob] offset:2048 sc1\n\t" \
  "Lp5_s2_%=:\n\t" \
  "v_cmp_eq_u32 vcc, -1, v139\n\t" \
  "s_cbranch_vccz Lp5_s3_%=\n\t" \
  "global_load_dwordx4 v[12:15], %[o0], %[ob] offset:3072 sc1\n\t" \
  "Lp5_s3_%=:\n\t" \
  "v_cmp_eq_u32 vcc, -1, v140\n\t" \
  "s_cbranch_vccz Lp5_s4_%=\n\t" \
  "global_load_dwordx4 v[16:19], %[o1], %[ob] offset:0 sc1\n\t" \
  "Lp5_s4_%=:\n\t" \
  "v_cmp_eq_u32 vcc, -1, v141\n\t" \
  "s_cbranch_vccz Lp5_s5_%=\n\t" \
  "global_load_dwordx4 v[20:23], %[o1], %[ob] offset:1024 sc1\n\t" \
  "Lp5_s5_%=:\n\t" \
  "v_cmp_eq_u32 vcc, -1, v142\n\t" \
  "s_cbranch_vccz Lp5_s6_%=\n\t" \
  "global_load_dwordx4 v[24:27], %[o1], %[ob] offset:2048 sc1\n\t" \
  "Lp5_s6_%=:\n\t" \
  "v_cmp_eq_u32 vcc, -1, v143\n\t" \
  "s_cbranch_vccz Lp5_s7_%=\n\t" \
  "global_load_dwordx4 v[28:31], %[o1], %[ob] offset:3072 sc1\n\t" \
  "Lp5_s7_%=:\n\t" \
  "s_waitcnt vmcnt(0)\n\t" \
  "v_max3_u32 v136, v0, v1, v2\n\t" \
  "v_max_u32 v136, v136, v3\n\t" \
  "v_max3_u32 v137, v4, v5, v6\n\t" \
  "v_max_u32 v137, v137, v7\n\t" \
  "v_max3_u32 v138, v8, v9, v10\n\t" \
  "v_max_u32 v138, v138, v11\n\t" \
  "v_max3_u32 v139, v12, v13, v14\n\t" \
  "v_max_u32 v139, v139, v15\n\t" \
  "v_max3_u32 v140, v16, v17, v18\n\t" \
  "v_max_u32 v140, v140, v19\n\t" \
  "v_max3_u32 v141, v20, v21, v22\n\t" \
  "v_max_u32 v141, v141, v23\n\t" \
  "v_max3_u32 v142, v24, v25, v26\n\t" \
  "v_max_u32 v142, v142, v27\n\t" \
  "v_max3_u32 v143, v28, v29, v30\n\t" \
  "v_max_u32 v143, v143, v31\n\t" \
  "v_max3_u32 v144, v136, v137, v138\n\t" \
  "v_max3_u32 v145, v139, v140, v141\n\t" \
  "v_max3_u32 v144, v144, v142, v143\n\t" \
  "v_max_u32 v144, v144, v145\n\t" \
  "v_cmp_eq_u32 vcc, -1, v144\n\t" \
  "s_cbranch_vccnz Lp5_poll_%=\n\t" \
  "Lp5_go_%=:\n\t" \
  "v_mfma_f32_32x32x16_bf16 v[96:111], v[32:35], v[0:3], 0\n\t" \
  "v_mfma_f32_32x32x16_bf16 v[112:127], v[36:39], v[0:3], 0\n\t" \
  "v_mfma_f32_32x32x16_bf16 v[96:111], v[40:43], v[4:7], v[96:111]\n\t" \
  "v_mfma_f32_32x32x16_bf16 v[112:127], v[44:47], v[4:7], v[112:127]\n\t" \
  "v_mfma_f32_32x32x16_bf16 v[96:111], v[48:51], v[8:11], v[96:111]\n\t" \
  "v_mfma_f32_32x32x16_bf16 v[112:127], v[52:55], v[8:11], v[112:127]\n\t" \
  "v_mfma_f32_32x32x16_bf16 v[96:111], v[56:59], v[12:15], v[96:111]\n\t" \
  "v_mfma_f32_32x32x16_bf16 v[112:127], v[60:63], v[12:15], v[112:127]\n\t" \
  "v_mfma_f32_32x32x16_bf16 v[96:111], v[64:67], v[16:19], v[96:111]\n\t" \
  "v_mfma_f32_32x32x16_bf16 v[112:127], v[68:71], v[16:19], v[112:127]\n\t" \
  "v_mfma_f32_32x32x16_bf16 v[96:111], v[72:75], v[20:23], v[96:111]\n\t" \
  "v_mfma_f32_32x32x16_bf16 v[112:127], v[76:79], v[20:23], v[112:127]\n\t" \
  "v_mfma_f32_32x32x16_bf16 v[96:111], v[80:83], v[24:27], v[96:111]\n\t" \
  "v_mfma_f32_32x32x16_bf16 v[112:127], v[84:87], v[24:27], v[112:127]\n\t" \
  "v_mfma_f32_32x32x16_bf16 v[96:111], v[88:91], v[28:31], v[96:111]\n\t" \
  "v_mfma_f32_32x32x16_bf16 v[112:127], v[92:95], v[28:31], v[112:127]\n\t" \
  "Lp5_done_%=:\n\t" \
  "s_nop 15\n\t" \
  "s_nop 3\n\t"
#define P5_CLOBBER "v32", "v33", "v34", "v35", "v36", "v37", "v38", "v39", "v40", "v41", "v42", "v43", "v44", "v45", "v46", "v47", "v48", "v49", "v50", "v51", "v52", "v53", "v54", "v55", "v56", "v57", "v58", "v59", "v60", "v61", "v62", "v63", "v64", "v65", "v66", "v67", "v68", "v69", "v70", "v71", "v72", "v73", "v74", "v75", "v76", "v77", "v78", "v79", "v80", "v81", "v82", "v83", "v84", "v85", "v86", "v87", "v88", "v89", "v90", "v91", "v92", "v93", "v94", "v95", "v136", "v137", "v138", "v139", "v140", "v141", "v142", "v143", "v144", "v145"
#define P5_LT
#define AT_CHUNK_ASM \
  "s_waitcnt lgkmcnt(0)\n\t" \
  "v_exp_f32 v48, v48\n\t" \
  "v_cvt_pk_bf16_f32 v75, v78, v79\n\t" \
  "v_exp_f32 v49, v49\n\t" \
  "v_exp_f32 v50, v50\n\t" \
  "v_mfma_f32_32x32x16_bf16 v[32:47], v[104:107], v[72:75], v[32:47]\n\t" \
  "v_exp_f32 v51, v51\n\t" \
  "v_exp_f32 v52, v52\n\t" \
  "v_exp_f32 v53, v53\n\t" \
  "v_exp_f32 v54, v54\n\t" \
  "v_mfma_f32_32x32x16_bf16 v[64:79], v[88:91], v[80:83], v[0:15]\n\t" \
  "ds_read_b128 v[84:87], %[ka] offset:2048\n\t" \
  "v_exp_f32 v55, v55\n\t" \
  "s_mov_b64 exec, %[em]\n\t" \
  "ds_read_b128 v[100:103], %[va] offset:1088\n\t" \
  "ds_read_b128 v[104:107], %[va] offset:1632\n\t" \
  "s_mov_b64 exec, -1\n\t" \
  "v_cvt_pk_bf16_f32 v48, v48, v49\n\t" \
  "v_cvt_pk_bf16_f32 v49, v50, v51\n\t" \
  "v_cvt_pk_bf16_f32 v50, v52, v53\n\t" \
  "v_cvt_pk_bf16_f32 v51, v54, v55\n\t" \
  "v_exp_f32 v56, v56\n\t" \
  "v_exp_f32 v57, v57\n\t" \
  "v_mfma_f32_32x32x16_bf16 v[16:31], v[92:95], v[48:51], v[16:31]\n\t" \
  "v_exp_f32 v58, v58\n\t" \
  "v_exp_f32 v59, v59\n\t" \
  "v_cvt_pk_bf16_f32 v56, v56, v57\n\t" \
  "v_exp_f32 v60, v60\n\t" \
  "v_exp_f32 v61, v61\n\t" \
  "v_cvt_pk_bf16_f32 v57, v58, v59\n\t" \
  "v_exp_f32 v62, v62\n\t" \
  "v_exp_f32 v63, v63\n\t" \
  "v_cvt_pk_bf16_f32 v58, v60, v61\n\t" \
  "s_waitcnt lgkmcnt(0)\n\t" \
  "v_exp_f32 v64, v64\n\t" \
  "v_cvt_pk_bf16_f32 v59, v62, v63\n\t" \
  "v_exp_f32 v65, v65\n\t" \
  "v_exp_f32 v66, v66\n\t" \
  "v_mfma_f32_32x32x16_bf16 v[32:47], v[96:99], v[56:59], v[32:47]\n\t" \
  "v_exp_f32 v67, v67\n\t" \
  "v_exp_f32 v68, v68\n\t" \
  "v_exp_f32 v69, v69\n\t" \
  "v_exp_f32 v70, v70\n\t" \
  "v_mfma_f32_32x32x16_bf16 v[48:63], v[84:87], v[80:83], v[0:15]\n\t" \
  "ds_read_b128 v[88:91], %[ka] offset:3072\n\t" \
  "v_exp_f32 v71, v71\n\t" \
  "s_mov_b64 exec, %[em]\n\t" \
  "ds_read_b128 v[92:95], %[va] offset:2176\n\t" \
  "ds_read_b128 v[96:99], %[va] offset:2720\n\t" \
  "s_mov_b64 exec, -1\n\t" \
  "v_cvt_pk_bf16_f32 v64, v64, v65\n\t" \
  "v_cvt_pk_bf16_f32 v65, v66, v67\n\t" \
  "v_cvt_pk_bf16_f32 v66, v68, v69\n\t" \
  "v_cvt_pk_bf16_f32 v67, v70, v71\n\t" \
  "v_exp_f32 v72, v72\n\t" \
  "v_exp_f32 v73, v73\n\t" \
  "v_mfma_f32_32x32x16_bf16 v[16:31], v[100:103], v[64:67], v[16:31]\n\t" \
  "v_exp_f32 v74, v74\n\t" \
  "v_exp_f32 v75, v75\n\t" \
  "v_cvt_pk_bf16_f32 v72, v72, v73\n\t" \
  "v_exp_f32 v76, v76\n\t" \
  "v_exp_f32 v77, v77\n\t" \
  "v_cvt_pk_bf16_f32 v73, v74, v75\n\t" \
  "v_exp_f32 v78, v78\n\t" \
  "v_exp_f32 v79, v79\n\t" \
  "v_cvt_pk_bf16_f32 v74, v76, v77\n\t" \
  "s_waitcnt lgkmcnt(0)\n\t" \
  "v_exp_f32 v48, v48\n\t" \
  "v_cvt_pk_bf16_f32 v75, v78, v79\n\t" \
  "v_exp_f32 v49, v49\n\t" \
  "v_exp_f32 v50, v50\n\t" \
  "v_mfma_f32_32x32x16_bf16 v[32:47], v[104:107], v[72:75], v[32:47]\n\t" \
  "v_exp_f32 v51, v51\n\t" \
  "v_exp_f32 v52, v52\n\t" \
  "v_exp_f32 v53, v53\n\t" \
  "v_exp_f32 v54, v54\n\t" \
  "v_mfma_f32_32x32x16_bf16 v[64:79], v[88:91], v[80:83], v[0:15]\n\t" \
  "ds_read_b128 v[84:87], %[ka] offset:4096\n\t" \
  "v_exp_f32 v55, v55\n\t" \
  "s_mov_b64 exec, %[em]\n\t" \
  "ds_read_b128 v[100:103], %[va] offset:3264\n\t" \
  "ds_read_b128 v[104:107], %[va] offset:3808\n\t" \
  "s_mov_b64 exec, -1\n\t" \
  "v_cvt_pk_bf16_f32 v48, v48, v49\n\t" \
  "v_cvt_pk_bf16_f32 v49, v50, v51\n\t" \
  "v_cvt_pk_bf16_f32 v50, v52, v53\n\t" \
  "v_cvt_pk_bf16_f32 v51, v54, v55\n\t" \
  "v_exp_f32 v56, v56\n\t" \
  "v_exp_f32 v57, v57\n\t" \
  "v_mfma_f32_32x32x16_bf16 v[16:31], v[92:95], v[48:51], v[16:31]\n\t" \
  "v_exp_f32 v58, v58\n\t" \
  "v_exp_f32 v59, v59\n\t" \
  "v_cvt_pk_bf16_f32 v56, v56, v57\n\t" \
  "v_exp_f32 v60, v60\n\t" \
  "v_exp_f32 v61, v61\n\t" \
  "v_cvt_pk_bf16_f32 v57, v58, v59\n\t" \
  "v_exp_f32 v62, v62\n\t" \
  "v_exp_f32 v63, v63\n\t" \
  "v_cvt_pk_bf16_f32 v58, v60, v61\n\t" \
  "s_waitcnt lgkmcnt(0)\n\t" \
  "v_exp_f32 v64, v64\n\t" \
  "v_cvt_pk_bf16_f32 v59, v62, v63\n\t" \
  "v_exp_f32 v65, v65\n\t" \
  "v_exp_f32 v66, v66\n\t" \
  "v_mfma_f32_32x32x16_bf16 v[32:47], v[96:99], v[56:59], v[32:47]\n\t" \
  "v_exp_f32 v67, v67\n\t" \
  "v_exp_f32 v68, v68\n\t" \
  "v_exp_f32 v69, v69\n\t" \
  "v_exp_f32 v70, v70\n\t" \
  "v_mfma_f32_32x32x16_bf16 v[48:63], v[84:87], v[80:83], v[0:15]\n\t" \
  "ds_read_b128 v[88:91], %[ka] offset:5120\n\t" \
  "v_exp_f32 v71, v71\n\t" \
  "s_mov_b64 exec, %[em]\n\t" \
  "ds_read_b128 v[92:95], %[va] offset:4352\n\t" \
  "ds_read_b128 v[96:99], %[va] offset:4896\n\t" \
  "s_mov_b64 exec, -1\n\t" \
  "v_cvt_pk_bf16_f32 v64, v64, v65\n\t" \
  "v_cvt_pk_bf16_f32 v65, v66, v67\n\t" \
  "v_cvt_pk_bf16_f32 v66, v68, v69\n\t" \
  "v_cvt_pk_bf16_f32 v67, v70, v71\n\t" \
  "v_exp_f32 v72, v72\n\t" \
  "v_exp_f32 v73, v73\n\t" \
  "v_mfma_f32_32x32x16_bf16 v[16:31], v[100:103], v[64:67], v[16:31]\n\t" \
  "v_exp_f32 v74, v74\n\t" \
  "v_exp_f32 v75, v75\n\t" \
  "v_cvt_pk_bf16_f32 v72, v72, v73\n\t" \
  "v_exp_f32 v76, v76\n\t" \
  "v_exp_f32 v77, v77\n\t" \
  "v_cvt_pk_bf16_f32 v73, v74, v75\n\t" \
  "v_exp_f32 v78, v78\n\t" \
  "v_exp_f32 v79, v79\n\t" \
  "v_cvt_pk_bf16_f32 v74, v76, v77\n\t" \
  "s_waitcnt lgkmcnt(0)\n\t" \
  "v_exp_f32 v48, v48\n\t" \
  "v_cvt_pk_bf16_f32 v75, v78, v79\n\t" \
  "v_exp_f32 v49, v49\n\t" \
  "v_exp_f32 v50, v50\n\t" \
  "v_mfma_f32_32x32x16_bf16 v[32:47], v[104:107], v[72:75], v[32:47]\n\t" \
  "v_exp_f32 v51, v51\n\t" \
  "v_exp_f32 v52, v52\n\t" \
  "v_exp_f32 v53, v53\n\t" \
  "v_exp_f32 v54, v54\n\t" \
  "v_mfma_f32_32x32x16_bf16 v[64:79], v[88:91], v[80:83], v[0:15]\n\t" \
  "ds_read_b128 v[84:87], %[ka] offset:6144\n\t" \
  "v_exp_f32 v55, v55\n\t" \
  "s_mov_b64 exec, %[em]\n\t" \
  "ds_read_b128 v[100:103], %[va] offset:5440\n\t" \
  "ds_read_b128 v[104:107], %[va] offset:5984\n\t" \
  "s_mov_b64 exec, -1\n\t" \
  "v_cvt_pk_bf16_f32 v48, v48, v49\n\t" \
  "v_cvt_pk_bf16_f32 v49, v50, v51\n\t" \
  "v_cvt_pk_bf16_f32 v50, v52, v53\n\t" \
  "v_cvt_pk_bf16_f32 v51, v54, v55\n\t" \
  "v_exp_f32 v56, v56\n\t" \
  "v_exp_f32 v57, v57\n\t" \
  "v_mfma_f32_32x32x16_bf16 v[16:31], v[92:95], v[48:51], v[16:31]\n\t" \
  "v_exp_f32 v58, v58\n\t" \
  "v_exp_f32 v59, v59\n\t" \
  "v_cvt_pk_bf16_f32 v56, v56, v57\n\t" \
  "v_exp_f32 v60, v60\n\t" \
  "v_exp_f32 v61, v61\n\t" \
  "v_cvt_pk_bf16_f32 v57, v58, v59\n\t" \
  "v_exp_f32 v62, v62\n\t" \
  "v_exp_f32 v63, v63\n\t" \
  "v_cvt_pk_bf16_f32 v58, v60, v61\n\t" \
  "s_waitcnt lgkmcnt(0)\n\t" \
  "v_exp_f32 v64, v64\n\t" \
  "v_cvt_pk_bf16_f32 v59, v62, v63\n\t" \
  "v_exp_f32 v65, v65\n\t" \
  "v_exp_f32 v66, v66\n\t" \
  "v_mfma_f32_32x32x16_bf16 v[32:47], v[96:99], v[56:59], v[32:47]\n\t" \
  "v_exp_f32 v67, v67\n\t" \
  "v_exp_f32 v68, v68\n\t" \
  "v_exp_f32 v69, v69\n\t" \
  "v_exp_f32 v70, v70\n\t" \
  "v_mfma_f32_32x32x16_bf16 v[48:63], v[84:87], v[80:83], v[0:15]\n\t" \
  "ds_read_b128 v[88:91], %[ka] offset:7168\n\t" \
  "v_exp_f32 v71, v71\n\t" \
  "s_mov_b64 exec, %[em]\n\t" \
  "ds_read_b128 v[92:95], %[va] offset:6528\n\t" \
  "ds_read_b128 v[96:99], %[va] offset:7072\n\t" \
  "s_mov_b64 exec, -1\n\t" \
  "v_cvt_pk_bf16_f32 v64, v64, v65\n\t" \
  "v_cvt_pk_bf16_f32 v65, v66, v67\n\t" \
  "v_cvt_pk_bf16_f32 v66, v68, v69\n\t" \
  "v_cvt_pk_bf16_f32 v67, v70, v71\n\t" \
  "v_exp_f32 v72, v72\n\t" \
  "v_exp_f32 v73, v73\n\t" \
  "v_mfma_f32_32x32x16_bf16 v[16:31], v[100:103], v[64:67], v[16:31]\n\t" \
  "v_exp_f32 v74, v74\n\t" \
  "v_exp_f32 v75, v75\n\t" \
  "v_cvt_pk_bf16_f32 v72, v72, v73\n\t" \
  "v_exp_f32 v76, v76\n\t" \
  "v_exp_f32 v77, v77\n\t" \
  "v_cvt_pk_bf16_f32 v73, v74, v75\n\t" \
  "v_exp_f32 v78, v78\n\t" \
  "v_exp_f32 v79, v79\n\t" \
  "v_cvt_pk_bf16_f32 v74, v76, v77\n\t" \
  "s_waitcnt lgkmcnt(0)\n\t" \
  "s_barrier\n\t" \
  "v_exp_f32 v48, v48\n\t" \
  "v_cvt_pk_bf16_f32 v75, v78, v79\n\t" \
  "v_exp_f32 v49, v49\n\t" \
  "v_exp_f32 v50, v50\n\t" \
  "v_mfma_f32_32x32x16_bf16 v[32:47], v[104:107], v[72:75], v[32:47]\n\t" \
  "v_exp_f32 v51, v51\n\t" \
  "v_exp_f32 v52, v52\n\t" \
  "v_exp_f32 v53, v53\n\t" \
  "v_exp_f32 v54, v54\n\t" \
  "v_mfma_f32_32x32x16_bf16 v[64:79], v[88:91], v[80:83], v[0:15]\n\t" \
  "ds_read_b128 v[84:87], %[kn] offset:0\n\t" \
  "v_exp_f32 v55, v55\n\t" \
  "s_mov_b64 exec, %[em]\n\t" \
  "ds_read_b128 v[100:103], %[va] offset:7616\n\t" \
  "ds_read_b128 v[104:107], %[va] offset:8160\n\t" \
  "s_mov_b64 exec, -1\n\t" \
  "v_cvt_pk_bf16_f32 v48, v48, v49\n\t" \
  "v_cvt_pk_bf16_f32 v49, v50, v51\n\t" \
  "v_cvt_pk_bf16_f32 v50, v52, v53\n\t" \
  "v_cvt_pk_bf16_f32 v51, v54, v55\n\t" \
  "v_exp_f32 v56, v56\n\t" \
  "v_exp_f32 v57, v57\n\t" \
  "v_mfma_f32_32x32x16_bf16 v[16:31], v[92:95], v[48:51], v[16:31]\n\t" \
  "v_exp_f32 v58, v58\n\t" \
  "v_exp_f32 v59, v59\n\t" \
  "v_cvt_pk_bf16_f32 v56, v56, v57\n\t" \
  "v_exp_f32 v60, v60\n\t" \
  "v_exp_f32 v61, v61\n\t" \
  "v_cvt_pk_bf16_f32 v57, v58, v59\n\t" \
  "v_exp_f32 v62, v62\n\t" \
  "v_exp_f32 v63, v63\n\t" \
  "v_cvt_pk_bf16_f32 v58, v60, v61\n\t" \
  "s_waitcnt lgkmcnt(0)\n\t" \
  "v_exp_f32 v64, v64\n\t" \
  "v_cvt_pk_bf16_f32 v59, v62, v63\n\t" \
  "v_exp_f32 v65, v65\n\t" \
  "v_exp_f32 v66, v66\n\t" \
  "v_mfma_f32_32x32x16_bf16 v[32:47], v[96:99], v[56:59], v[32:47]\n\t" \
  "v_exp_f32 v67, v67\n\t" \
  "v_exp_f32 v68, v68\n\t" \
  "v_exp_f32 v69, v69\n\t" \
  "v_exp_f32 v70, v70\n\t" \
  "v_mfma_f32_32x32x16_bf16 v[48:63], v[84:87], v[80:83], v[0:15]\n\t" \
  "ds_read_b128 v[88:91], %[kn] offset:1024\n\t" \
  "v_exp_f32 v71, v71\n\t" \
  "s_mov_b64 exec, %[em]\n\t" \
  "ds_read_b128 v[92:95], %[vn]\n\t" \
  "ds_read_b128 v[96:99], %[vn] offset:544\n\t" \
  "s_mov_b64 exec, -1\n\t" \
  "v_cvt_pk_bf16_f32 v64, v64, v65\n\t" \
  "v_cvt_pk_bf16_f32 v65, v66, v67\n\t" \
  "v_cvt_pk_bf16_f32 v66, v68, v69\n\t" \
  "v_cvt_pk_bf16_f32 v67, v70, v71\n\t" \
  "v_exp_f32 v72, v72\n\t" \
  "v_exp_f32 v73, v73\n\t" \
  "v_mfma_f32_32x32x16_bf16 v[16:31], v[100:103], v[64:67], v[16:31]\n\t" \
  "v_exp_f32 v74, v74\n\t" \
  "v_exp_f32 v75, v75\n\t" \
  "v_cvt_pk_bf16_f32 v72, v72, v73\n\t" \
  "v_exp_f32 v76, v76\n\t" \
  "v_exp_f32 v77, v77\n\t" \
  "v_cvt_pk_bf16_f32 v73, v74, v75\n\t" \
  "v_exp_f32 v78, v78\n\t" \
  "v_exp_f32 v79, v79\n\t" \
  "v_cvt_pk_bf16_f32 v74, v76, v77\n\t"
#define AT_TAIL_ASM \
  "s_nop 1\n\t" \
  "v_cvt_pk_bf16_f32 v75, v78, v79\n\t" \
  "s_nop 1\n\t" \
  "v_mfma_f32_32x32x16_bf16 v[32:47], v[104:107], v[72:75], v[32:47]\n\t" \
  "s_waitcnt lgkmcnt(0)\n\t" \
  "s_nop 15\n\t" \
  "s_nop 3\n\t"
#define AT_AUX_ASM \
  "s_setprio 3\n\t" \
  "s_add_u32 m0, %[lk], 16896\n\t" \
  "s_nop 0\n\t" \
  "global_load_lds_dwordx4 v152, %[kg]\n\t" \
  "global_load_lds_dwordx4 v152, %[kg] offset:1024\n\t" \
  "s_add_u32 m0, %[lk], 25088\n\t" \
  "v_add_u32 v152, 8192, v152\n\t" \
  "global_load_lds_dwordx4 v153, %[vg]\n\t" \
  "global_load_lds_dwordx4 v153, %[vg] offset:1024\n\t" \
  "s_add_u32 m0, %[lt], 16896\n\t" \
  "s_mov_b64 exec, 0xff\n\t" \
  "global_load_lds_dwordx4 v154, %[vg]\n\t" \
  "s_mov_b64 exec, -1\n\t" \
  "v_add_u32 v153, 0x2200, v153\n\t" \
  "v_add_u32 v154, 0x2200, v154\n\t" \
  "s_barrier\n\t" \
  "v_add_u32 v144, 0, %[dw]\n\t" \
  "v_add_u32 v145, 8192, %[dw]\n\t" \
  "v_add_u32 v146, 16384, %[dw]\n\t" \
  "v_add_u32 v147, 24576, %[dw]\n\t" \
  "v_add_u32 v148, 32768, %[dw]\n\t" \
  "v_add_u32 v149, 40960, %[dw]\n\t" \
  "v_add_u32 v150, 49152, %[dw]\n\t" \
  "v_add_u32 v151, 57344, %[dw]\n\t" \
  "global_load_dwordx4 v[64:67], v155, %[h0] offset:0\n\t" \
  "global_load_dwordx4 v[68:71], v155, %[h1] offset:0\n\t" \
  "global_load_dwordx4 v[72:75], v155, %[h0] offset:1024\n\t" \
  "global_load_dwordx4 v[76:79], v155, %[h1] offset:1024\n\t" \
  "global_load_dwordx4 v[80:83], v155, %[h0] offset:2048\n\t" \
  "global_load_dwordx4 v[84:87], v155, %[h1] offset:2048\n\t" \
  "global_load_dwordx4 v[88:91], v155, %[h0] offset:3072\n\t" \
  "global_load_dwordx4 v[92:95], v155, %[h1] offset:3072\n\t" \
  "v_add_u32 v155, 4096, v155\n\t" \
  "s_add_u32 m0, %[lw], 0\n\t" \
  "s_nop 0\n\t" \
  "global_load_lds_dwordx4 v144, %[wd]\n\t" \
  "s_add_u32 m0, %[lw], 1024\n\t" \
  "s_nop 0\n\t" \
  "global_load_lds_dwordx4 v145, %[wd]\n\t" \
  "s_add_u32 m0, %[lw], 2048\n\t" \
  "s_nop 0\n\t" \
  "global_load_lds_dwordx4 v146, %[wd]\n\t" \
  "s_add_u32 m0, %[lw], 3072\n\t" \
  "s_nop 0\n\t" \
  "global_load_lds_dwordx4 v147, %[wd]\n\t" \
  "s_add_u32 m0, %[lw], 4096\n\t" \
  "s_nop 0\n\t" \
  "global_load_lds_dwordx4 v148, %[wd]\n\t" \
  "s_add_u32 m0, %[lw], 5120\n\t" \
  "s_nop 0\n\t" \
  "global_load_lds_dwordx4 v149, %[wd]\n\t" \
  "s_add_u32 m0, %[lw], 6144\n\t" \
  "s_nop 0\n\t" \
  "global_load_lds_dwordx4 v150, %[wd]\n\t" \
  "s_add_u32 m0, %[lw], 7168\n\t" \
  "s_nop 0\n\t" \
  "global_load_lds_dwordx4 v151, %[wd]\n\t" \
  "v_add_u32 v144, 0x10000, v144\n\t" \
  "v_add_u32 v145, 0x10000, v145\n\t" \
  "v_add_u32 v146, 0x10000, v146\n\t" \
  "v_add_u32 v147, 0x10000, v147\n\t" \
  "v_add_u32 v148, 0x10000, v148\n\t" \
  "v_add_u32 v149, 0x10000, v149\n\t" \
  "v_add_u32 v150, 0x10000, v150\n\t" \
  "v_add_u32 v151, 0x10000, v151\n\t" \
  "v_mov_b32 v0, 0\n\t" \
  "v_mov_b32 v1, 0\n\t" \
  "v_mov_b32 v2, 0\n\t" \
  "v_mov_b32 v3, 0\n\t" \
  "v_mov_b32 v4, 0\n\t" \
  "v_mov_b32 v5, 0\n\t" \
  "v_mov_b32 v6, 0\n\t" \
  "v_mov_b32 v7, 0\n\t" \
  "v_mov_b32 v8, 0\n\t" \
  "v_mov_b32 v9, 0\n\t" \
  "v_mov_b32 v10, 0\n\t" \
  "v_mov_b32 v11, 0\n\t" \
  "v_mov_b32 v12, 0\n\t" \
  "v_mov_b32 v13, 0\n\t" \
  "v_mov_b32 v14, 0\n\t" \
  "v_mov_b32 v15, 0\n\t" \
  "v_mov_b32 v16, 0\n\t" \
  "v_mov_b32 v17, 0\n\t" \
  "v_mov_b32 v18, 0\n\t" \
  "v_mov_b32 v19, 0\n\t" \
  "v_mov_b32 v20, 0\n\t" \
  "v_mov_b32 v21, 0\n\t" \
  "v_mov_b32 v22, 0\n\t" \
  "v_mov_b32 v23, 0\n\t" \
  "v_mov_b32 v24, 0\n\t" \
  "v_mov_b32 v25, 0\n\t" \
  "v_mov_b32 v26, 0\n\t" \
  "v_mov_b32 v27, 0\n\t" \
  "v_mov_b32 v28, 0\n\t" \
  "v_mov_b32 v29, 0\n\t" \
  "v_mov_b32 v30, 0\n\t" \
  "v_mov_b32 v31, 0\n\t" \
  "v_mov_b32 v32, 0\n\t" \
  "v_mov_b32 v33, 0\n\t" \
  "v_mov_b32 v34, 0\n\t" \
  "v_mov_b32 v35, 0\n\t" \
  "v_mov_b32 v36, 0\n\t" \
  "v_mov_b32 v37, 0\n\t" \
  "v_mov_b32 v38, 0\n\t" \
  "v_mov_b32 v39, 0\n\t" \
  "v_mov_b32 v40, 0\n\t" \
  "v_mov_b32 v41, 0\n\t" \
  "v_mov_b32 v42, 0\n\t" \
  "v_mov_b32 v43, 0\n\t" \
  "v_mov_b32 v44, 0\n\t" \
  "v_mov_b32 v45, 0\n\t" \
  "v_mov_b32 v46, 0\n\t" \
  "v_mov_b32 v47, 0\n\t" \
  "v_mov_b32 v48, 0\n\t" \
  "v_mov_b32 v49, 0\n\t" \
  "v_mov_b32 v50, 0\n\t" \
  "v_mov_b32 v51, 0\n\t" \
  "v_mov_b32 v52, 0\n\t" \
  "v_mov_b32 v53, 0\n\t" \
  "v_mov_b32 v54, 0\n\t" \
  "v_mov_b32 v55, 0\n\t" \
  "v_mov_b32 v56, 0\n\t" \
  "v_mov_b32 v57, 0\n\t" \
  "v_mov_b32 v58, 0\n\t" \
  "v_mov_b32 v59, 0\n\t" \
  "v_mov_b32 v60, 0\n\t" \
  "v_mov_b32 v61, 0\n\t" \
  "v_mov_b32 v62, 0\n\t" \
  "v_mov_b32 v63, 0\n\t" \
  "s_waitcnt vmcnt(16)\n\t" \
  "s_barrier\n\t" \
  "s_add_u32 m0, %[lk], 33792\n\t" \
  "s_nop 0\n\t" \
  "global_load_lds_dwordx4 v152, %[kg]\n\t" \
  "global_load_lds_dwordx4 v152, %[kg] offset:1024\n\t" \
  "s_add_u32 m0, %[lk], 41984\n\t" \
  "v_add_u32 v152, 8192, v152\n\t" \
  "global_load_lds_dwordx4 v153, %[vg]\n\t" \
  "global_load_lds_dwordx4 v153, %[vg] offset:1024\n\t" \
  "s_add_u32 m0, %[lt], 33792\n\t" \
  "s_mov_b64 exec, 0xff\n\t" \
  "global_load_lds_dwordx4 v154, %[vg]\n\t" \
  "s_mov_b64 exec, -1\n\t" \
  "v_add_u32 v153, 0x2200, v153\n\t" \
  "v_add_u32 v154, 0x2200, v154\n\t" \
  "global_load_dwordx4 v[96:99], v155, %[h0] offset:0\n\t" \
  "global_load_dwordx4 v[100:103], v155, %[h1] offset:0\n\t" \
  "global_load_dwordx4 v[104:107], v155, %[h0] offset:1024\n\t" \
  "global_load_dwordx4 v[108:111], v155, %[h1] offset:1024\n\t" \
  "global_load_dwordx4 v[112:115], v155, %[h0] offset:2048\n\t" \
  "global_load_dwordx4 v[116:119], v155, %[h1] offset:2048\n\t" \
  "global_load_dwordx4 v[120:123], v155, %[h0] offset:3072\n\t" \
  "global_load_dwordx4 v[124:127], v155, %[h1] offset:3072\n\t" \
  "v_add_u32 v155, 4096, v155\n\t" \
  "s_add_u32 m0, %[lw], 8192\n\t" \
  "s_nop 0\n\t" \
  "global_load_lds_dwordx4 v144, %[wd]\n\t" \
  "s_add_u32 m0, %[lw], 9216\n\t" \
  "s_nop 0\n\t" \
  "global_load_lds_dwordx4 v145, %[wd]\n\t" \
  "s_add_u32 m0, %[lw], 10240\n\t" \
  "s_nop 0\n\t" \
  "global_load_lds_dwordx4 v146, %[wd]\n\t" \
  "s_add_u32 m0, %[lw], 11264\n\t" \
  "s_nop 0\n\t" \
  "global_load_lds_dwordx4 v147, %[wd]\n\t" \
  "s_add_u32 m0, %[lw], 12288\n\t" \
  "s_nop 0\n\t" \
  "global_load_lds_dwordx4 v148, %[wd]\n\t" \
  "s_add_u32 m0, %[lw], 13312\n\t" \
  "s_nop 0\n\t" \
  "global_load_lds_dwordx4 v149, %[wd]\n\t" \
  "s_add_u32 m0, %[lw], 14336\n\t" \
  "s_nop 0\n\t" \
  "global_load_lds_dwordx4 v150, %[wd]\n\t" \
  "s_add_u32 m0, %[lw], 15360\n\t" \
  "s_nop 0\n\t" \
  "global_load_lds_dwordx4 v151, %[wd]\n\t" \
  "v_add_u32 v144, 0x10000, v144\n\t" \
  "v_add_u32 v145, 0x10000, v145\n\t" \
  "v_add_u32 v146, 0x10000, v146\n\t" \
  "v_add_u32 v147, 0x10000, v147\n\t" \
  "v_add_u32 v148, 0x10000, v148\n\t" \
  "v_add_u32 v149, 0x10000, v149\n\t" \
  "v_add_u32 v150, 0x10000, v150\n\t" \
  "v_add_u32 v151, 0x10000, v151\n\t" \
  "s_waitcnt vmcnt(21)\n\t" \
  "s_setprio 0\n\t" \
  "ds_read_b64_tr_b16 v[128:129], %[r0] offset:0\n\t" \
  "ds_read_b64_tr_b16 v[130:131], %[r0] offset:1024\n\t" \
  "ds_read_b64_tr_b16 v[132:133], %[r1] offset:0\n\t" \
  "ds_read_b64_tr_b16 v[134:135], %[r1] offset:1024\n\t" \
  "ds_read_b64_tr_b16 v[136:137], %[r0] offset:2048\n\t" \
  "ds_read_b64_tr_b16 v[138:139], %[r0] offset:3072\n\t" \
  "s_waitcnt lgkmcnt(4)\n\t" \
  "v_mfma_f32_32x32x16_bf16 v[0:15], v[128:131], v[64:67], v[0:15]\n\t" \
  "v_mfma_f32_32x32x16_bf16 v[32:47], v[128:131], v[68:71], v[32:47]\n\t" \
  "ds_read_b64_tr_b16 v[140:141], %[r1] offset:2048\n\t" \
  "ds_read_b64_tr_b16 v[142:143], %[r1] offset:3072\n\t" \
  "s_waitcnt lgkmcnt(4)\n\t" \
  "v_mfma_f32_32x32x16_bf16 v[16:31], v[132:135], v[64:67], v[16:31]\n\t" \
  "v_mfma_f32_32x32x16_bf16 v[48:63], v[132:135], v[68:71], v[48:63]\n\t" \
  "ds_read_b64_tr_b16 v[128:129], %[r0] offset:4096\n\t" \
  "ds_read_b64_tr_b16 v[130:131], %[r0] offset:5120\n\t" \
  "s_waitcnt lgkmcnt(4)\n\t" \
  "v_mfma_f32_32x32x16_bf16 v[0:15], v[136:139], v[72:75], v[0:15]\n\t" \
  "v_mfma_f32_32x32x16_bf16 v[32:47], v[136:139], v[76:79], v[32:47]\n\t" \
  "ds_read_b64_tr_b16 v[132:133], %[r1] offset:4096\n\t" \
  "ds_read_b64_tr_b16 v[134:135], %[r1] offset:5120\n\t" \
  "s_waitcnt lgkmcnt(4)\n\t" \
  "v_mfma_f32_32x32x16_bf16 v[16:31], v[140:143], v[72:75], v[16:31]\n\t" \
  "v_mfma_f32_32x32x16_bf16 v[48:63], v[140:143], v[76:79], v[48:63]\n\t" \
  "ds_read_b64_tr_b16 v[136:137], %[r0] offset:6144\n\t" \
  "ds_read_b64_tr_b16 v[138:139], %[r0] offset:7168\n\t" \
  "s_waitcnt lgkmcnt(4)\n\t" \
  "v_mfma_f32_32x32x16_bf16 v[0:15], v[128:131], v[80:83], v[0:15]\n\t" \
  "v_mfma_f32_32x32x16_bf16 v[32:47], v[128:131], v[84:87], v[32:47]\n\t" \
  "ds_read_b64_tr_b16 v[140:141], %[r1] offset:6144\n\t" \
  "ds_read_b64_tr_b16 v[142:143], %[r1] offset:7168\n\t" \
  "s_waitcnt lgkmcnt(4)\n\t" \
  "v_mfma_f32_32x32x16_bf16 v[16:31], v[132:135], v[80:83], v[16:31]\n\t" \
  "v_mfma_f32_32x32x16_bf16 v[48:63], v[132:135], v[84:87], v[48:63]\n\t" \
  "s_waitcnt lgkmcnt(2)\n\t" \
  "v_mfma_f32_32x32x16_bf16 v[0:15], v[136:139], v[88:91], v[0:15]\n\t" \
  "v_mfma_f32_32x32x16_bf16 v[32:47], v[136:139], v[92:95], v[32:47]\n\t" \
  "s_waitcnt lgkmcnt(0)\n\t" \
  "v_mfma_f32_32x32x16_bf16 v[16:31], v[140:143], v[88:91], v[16:31]\n\t" \
  "v_mfma_f32_32x32x16_bf16 v[48:63], v[140:143], v[92:95], v[48:63]\n\t" \
  "s_setprio 3\n\t" \
  "global_load_dwordx4 v[64:67], v155, %[h0] offset:0\n\t" \
  "global_load_dwordx4 v[68:71], v155, %[h1] offset:0\n\t" \
  "global_load_dwordx4 v[72:75], v155, %[h0] offset:1024\n\t" \
  "global_load_dwordx4 v[76:79], v155, %[h1] offset:1024\n\t" \
  "global_load_dwordx4 v[80:83], v155, %[h0] offset:2048\n\t" \
  "global_load_dwordx4 v[84:87], v155, %[h1] offset:2048\n\t" \
  "global_load_dwordx4 v[88:91], v155, %[h0] offset:3072\n\t" \
  "global_load_dwordx4 v[92:95], v155, %[h1] offset:3072\n\t" \
  "v_add_u32 v155, 4096, v155\n\t" \
  "s_add_u32 m0, %[lw], 0\n\t" \
  "s_nop 0\n\t" \
  "global_load_lds_dwordx4 v144, %[wd]\n\t" \
  "s_add_u32 m0, %[lw], 1024\n\t" \
  "s_nop 0\n\t" \
  "global_load_lds_dwordx4 v145, %[wd]\n\t" \
  "s_add_u32 m0, %[lw], 2048\n\t" \
  "s_nop 0\n\t" \
  "global_load_lds_dwordx4 v146, %[wd]\n\t" \
  "s_add_u32 m0, %[lw], 3072\n\t" \
  "s_nop 0\n\t" \
  "global_load_lds_dwordx4 v147, %[wd]\n\t" \
  "s_add_u32 m0, %[lw], 4096\n\t" \
  "s_nop 0\n\t" \
  "global_load_lds_dwordx4 v148, %[wd]\n\t" \
  "s_add_u32 m0, %[lw], 5120\n\t" \
  "s_nop 0\n\t" \
  "global_load_lds_dwordx4 v149, %[wd]\n\t" \
  "s_add_u32 m0, %[lw], 6144\n\t" \
  "s_nop 0\n\t" \
  "global_load_lds_dwordx4 v150, %[wd]\n\t" \
  "s_add_u32 m0, %[lw], 7168\n\t" \
  "s_nop 0\n\t" \
  "global_load_lds_dwordx4 v151, %[wd]\n\t" \
  "v_add_u32 v144, 0x10000, v144\n\t" \
  "v_add_u32 v145, 0x10000, v145\n\t" \
  "v_add_u32 v146, 0x10000, v146\n\t" \
  "v_add_u32 v147, 0x10000, v147\n\t" \
  "v_add_u32 v148, 0x10000, v148\n\t" \
  "v_add_u32 v149, 0x10000, v149\n\t" \
  "v_add_u32 v150, 0x10000, v150\n\t" \
  "v_add_u32 v151, 0x10000, v151\n\t" \
  "s_waitcnt vmcnt(16)\n\t" \
  "s_barrier\n\t" \
  "s_add_u32 m0, %[lk], 0\n\t" \
  "s_nop 0\n\t" \
  "global_load_lds_dwordx4 v152, %[kg]\n\t" \
  "global_load_lds_dwordx4 v152, %[kg] offset:1024\n\t" \
  "s_add_u32 m0, %[lk], 8192\n\t" \
  "v_add_u32 v152, 8192, v152\n\t" \
  "global_load_lds_dwordx4 v153, %[vg]\n\t" \
  "global_load_lds_dwordx4 v153, %[vg] offset:1024\n\t" \
  "s_add_u32 m0, %[lt], 0\n\t" \
  "s_mov_b64 exec, 0xff\n\t" \
  "global_load_lds_dwordx4 v154, %[vg]\n\t" \
  "s_mov_b64 exec, -1\n\t" \
  "v_add_u32 v153, 0x2200, v153\n\t" \
  "v_add_u32 v154, 0x2200, v154\n\t" \
  "s_setprio 0\n\t" \
  "ds_read_b64_tr_b16 v[128:129], %[r0] offset:8192\n\t" \
  "ds_read_b64_tr_b16 v[130:131], %[r0] offset:9216\n\t" \
  "ds_read_b64_tr_b16 v[132:133], %[r1] offset:8192\n\t" \
  "ds_read_b64_tr_b16 v[134:135], %[r1] offset:9216\n\t" \
  "ds_read_b64_tr_b16 v[136:137], %[r0] offset:10240\n\t" \
  "ds_read_b64_tr_b16 v[138:139], %[r0] offset:11264\n\t" \
  "s_waitcnt lgkmcnt(4)\n\t" \
  "v_mfma_f32_32x32x16_bf16 v[0:15], v[128:131], v[96:99], v[0:15]\n\t" \
  "v_mfma_f32_32x32x16_bf16 v[32:47], v[128:131], v[100:103], v[32:47]\n\t" \
  "ds_read_b64_tr_b16 v[140:141], %[r1] offset:10240\n\t" \
  "ds_read_b64_tr_b16 v[142:143], %[r1] offset:11264\n\t" \
  "s_waitcnt lgkmcnt(4)\n\t" \
  "v_mfma_f32_32x32x16_bf16 v[16:31], v[132:135], v[96:99], v[16:31]\n\t" \
  "v_mfma_f32_32x32x16_bf16 v[48:63], v[132:135], v[100:103], v[48:63]\n\t" \
  "ds_read_b64_tr_b16 v[128:129], %[r0] offset:12288\n\t" \
  "ds_read_b64_tr_b16 v[130:131], %[r0] offset:13312\n\t" \
  "s_waitcnt lgkmcnt(4)\n\t" \
  "v_mfma_f32_32x32x16_bf16 v[0:15], v[136:139], v[104:107], v[0:15]\n\t" \
  "v_mfma_f32_32x32x16_bf16 v[32:47], v[136:139], v[108:111], v[32:47]\n\t" \
  "ds_read_b64_tr_b16 v[132:133], %[r1] offset:12288\n\t" \
  "ds_read_b64_tr_b16 v[134:135], %[r1] offset:13312\n\t" \
  "s_waitcnt lgkmcnt(4)\n\t" \
  "v_mfma_f32_32x32x16_bf16 v[16:31], v[140:143], v[104:107], v[16:31]\n\t" \
  "v_mfma_f32_32x32x16_bf16 v[48:63], v[140:143], v[108:111], v[48:63]\n\t" \
  "ds_read_b64_tr_b16 v[136:137], %[r0] offset:14336\n\t" \
  "ds_read_b64_tr_b16 v[138:139], %[r0] offset:15360\n\t" \
  "s_waitcnt lgkmcnt(4)\n\t" \
  "v_mfma_f32_32x32x16_bf16 v[0:15], v[128:131], v[112:115], v[0:15]\n\t" \
  "v_mfma_f32_32x32x16_bf16 v[32:47], v[128:131], v[116:119], v[32:47]\n\t" \
  "ds_read_b64_tr_b16 v[140:141], %[r1] offset:14336\n\t" \
  "ds_read_b64_tr_b16 v[142:143], %[r1] offset:15360\n\t" \
  "s_waitcnt lgkmcnt(4)\n\t" \
  "v_mfma_f32_32x32x16_bf16 v[16:31], v[132:135], v[112:115], v[16:31]\n\t" \
  "v_mfma_f32_32x32x16_bf16 v[48:63], v[132:135], v[116:119], v[48:63]\n\t" \
  "s_waitcnt lgkmcnt(2)\n\t" \
  "v_mfma_f32_32x32x16_bf16 v[0:15], v[136:139], v[120:123], v[0:15]\n\t" \
  "v_mfma_f32_32x32x16_bf16 v[32:47], v[136:139], v[124:127], v[32:47]\n\t" \
  "s_waitcnt lgkmcnt(0)\n\t" \
  "v_mfma_f32_32x32x16_bf16 v[16:31], v[140:143], v[120:123], v[16:31]\n\t" \
  "v_mfma_f32_32x32x16_bf16 v[48:63], v[140:143], v[124:127], v[48:63]\n\t" \
  "s_setprio 3\n\t" \
  "global_load_dwordx4 v[96:99], v155, %[h0] offset:0\n\t" \
  "global_load_dwordx4 v[100:103], v155, %[h1] offset:0\n\t" \
  "global_load_dwordx4 v[104:107], v155, %[h0] offset:1024\n\t" \
  "global_load_dwordx4 v[108:111], v155, %[h1] offset:1024\n\t" \
  "global_load_dwordx4 v[112:115], v155, %[h0] offset:2048\n\t" \
  "global_load_dwordx4 v[116:119], v155, %[h1] offset:2048\n\t" \
  "global_load_dwordx4 v[120:123], v155, %[h0] offset:3072\n\t" \
  "global_load_dwordx4 v[124:127], v155, %[h1] offset:3072\n\t" \
  "v_add_u32 v155, 4096, v155\n\t" \
  "s_add_u32 m0, %[lw], 8192\n\t" \
  "s_nop 0\n\t" \
  "global_load_lds_dwordx4 v144, %[wd]\n\t" \
  "s_add_u32 m0, %[lw], 9216\n\t" \
  "s_nop 0\n\t" \
  "global_load_lds_dwordx4 v145, %[wd]\n\t" \
  "s_add_u32 m0, %[lw], 10240\n\t" \
  "s_nop 0\n\t" \
  "global_load_lds_dwordx4 v146, %[wd]\n\t" \
  "s_add_u32 m0, %[lw], 11264\n\t" \
  "s_nop 0\n\t" \
  "global_load_lds_dwordx4 v147, %[wd]\n\t" \
  "s_add_u32 m0, %[lw], 12288\n\t" \
  "s_nop 0\n\t" \
  "global_load_lds_dwordx4 v148, %[wd]\n\t" \
  "s_add_u32 m0, %[lw], 13312\n\t" \
  "s_nop 0\n\t" \
  "global_load_lds_dwordx4 v149, %[wd]\n\t" \
  "s_add_u32 m0, %[lw], 14336\n\t" \
  "s_nop 0\n\t" \
  "global_load_lds_dwordx4 v150, %[wd]\n\t" \
  "s_add_u32 m0, %[lw], 15360\n\t" \
  "s_nop 0\n\t" \
  "global_load_lds_dwordx4 v151, %[wd]\n\t" \
  "v_add_u32 v144, 0x10000, v144\n\t" \
  "v_add_u32 v145, 0x10000, v145\n\t" \
  "v_add_u32 v146, 0x10000, v146\n\t" \
  "v_add_u32 v147, 0x10000, v147\n\t" \
  "v_add_u32 v148, 0x10000, v148\n\t" \
  "v_add_u32 v149, 0x10000, v149\n\t" \
  "v_add_u32 v150, 0x10000, v150\n\t" \
  "v_add_u32 v151, 0x10000, v151\n\t" \
  "s_waitcnt vmcnt(16)\n\t" \
  "s_barrier\n\t" \
  "s_add_u32 m0, %[lk], 16896\n\t" \
  "s_nop 0\n\t" \
  "global_load_lds_dwordx4 v152, %[kg]\n\t" \
  "global_load_lds_dwordx4 v152, %[kg] offset:1024\n\t" \
  "s_add_u32 m0, %[lk], 25088\n\t" \
  "v_add_u32 v152, 8192, v152\n\t" \
  "global_load_lds_dwordx4 v153, %[vg]\n\t" \
  "global_load_lds_dwordx4 v153, %[vg] offset:1024\n\t" \
  "s_add_u32 m0, %[lt], 16896\n\t" \
  "s_mov_b64 exec, 0xff\n\t" \
  "global_load_lds_dwordx4 v154, %[vg]\n\t" \
  "s_mov_b64 exec, -1\n\t" \
  "v_add_u32 v153, 0x2200, v153\n\t" \
  "v_add_u32 v154, 0x2200, v154\n\t" \
  "s_setprio 0\n\t" \
  "ds_read_b64_tr_b16 v[128:129], %[r0] offset:0\n\t" \
  "ds_read_b64_tr_b16 v[130:131], %[r0] offset:1024\n\t" \
  "ds_read_b64_tr_b16 v[132:133], %[r1] offset:0\n\t" \
  "ds_read_b64_tr_b16 v[134:135], %[r1] offset:1024\n\t" \
  "ds_read_b64_tr_b16 v[136:137], %[r0] offset:2048\n\t" \
  "ds_read_b64_tr_b16 v[138:139], %[r0] offset:3072\n\t" \
  "s_waitcnt lgkmcnt(4)\n\t" \
  "v_mfma_f32_32x32x16_bf16 v[0:15], v[128:131], v[64:67], v[0:15]\n\t" \
  "v_mfma_f32_32x32x16_bf16 v[32:47], v[128:131], v[68:71], v[32:47]\n\t" \
  "ds_read_b64_tr_b16 v[140:141], %[r1] offset:2048\n\t" \
  "ds_read_b64_tr_b16 v[142:143], %[r1] offset:3072\n\t" \
  "s_waitcnt lgkmcnt(4)\n\t" \
  "v_mfma_f32_32x32x16_bf16 v[16:31], v[132:135], v[64:67], v[16:31]\n\t" \
  "v_mfma_f32_32x32x16_bf16 v[48:63], v[132:135], v[68:71], v[48:63]\n\t" \
  "ds_read_b64_tr_b16 v[128:129], %[r0] offset:4096\n\t" \
  "ds_read_b64_tr_b16 v[130:131], %[r0] offset:5120\n\t" \
  "s_waitcnt lgkmcnt(4)\n\t" \
  "v_mfma_f32_32x32x16_bf16 v[0:15], v[136:139], v[72:75], v[0:15]\n\t" \
  "v_mfma_f32_32x32x16_bf16 v[32:47], v[136:139], v[76:79], v[32:47]\n\t" \
  "ds_read_b64_tr_b16 v[132:133], %[r1] offset:4096\n\t" \
  "ds_read_b64_tr_b16 v[134:135], %[r1] offset:5120\n\t" \
  "s_waitcnt lgkmcnt(4)\n\t" \
  "v_mfma_f32_32x32x16_bf16 v[16:31], v[140:143], v[72:75], v[16:31]\n\t" \
  "v_mfma_f32_32x32x16_bf16 v[48:63], v[140:143], v[76:79], v[48:63]\n\t" \
  "ds_read_b64_tr_b16 v[136:137], %[r0] offset:6144\n\t" \
  "ds_read_b64_tr_b16 v[138:139], %[r0] offset:7168\n\t" \
  "s_waitcnt lgkmcnt(4)\n\t" \
  "v_mfma_f32_32x32x16_bf16 v[0:15], v[128:131], v[80:83], v[0:15]\n\t" \
  "v_mfma_f32_32x32x16_bf16 v[32:47], v[128:131], v[84:87], v[32:47]\n\t" \
  "ds_read_b64_tr_b16 v[140:141], %[r1] offset:6144\n\t" \
  "ds_read_b64_tr_b16 v[142:143], %[r1] offset:7168\n\t" \
  "s_waitcnt lgkmcnt(4)\n\t" \
  "v_mfma_f32_32x32x16_bf16 v[16:31], v[132:135], v[80:83], v[16:31]\n\t" \
  "v_mfma_f32_32x32x16_bf16 v[48:63], v[132:135], v[84:87], v[48:63]\n\t" \
  "s_waitcnt lgkmcnt(2)\n\t" \
  "v_mfma_f32_32x32x16_bf16 v[0:15], v[136:139], v[88:91], v[0:15]\n\t" \
  "v_mfma_f32_32x32x16_bf16 v[32:47], v[136:139], v[92:95], v[32:47]\n\t" \
  "s_waitcnt lgkmcnt(0)\n\t" \
  "v_mfma_f32_32x32x16_bf16 v[16:31], v[140:143], v[88:91], v[16:31]\n\t" \
  "v_mfma_f32_32x32x16_bf16 v[48:63], v[140:143], v[92:95], v[48:63]\n\t" \
  "s_setprio 3\n\t" \
  "global_load_dwordx4 v[64:67], v155, %[h0] offset:0\n\t" \
  "global_load_dwordx4 v[68:71], v155, %[h1] offset:0\n\t" \
  "global_load_dwordx4 v[72:75], v155, %[h0] offset:1024\n\t" \
  "global_load_dwordx4 v[76:79], v155, %[h1] offset:1024\n\t" \
  "global_load_dwordx4 v[80:83], v155, %[h0] offset:2048\n\t" \
  "global_load_dwordx4 v[84:87], v155, %[h1] offset:2048\n\t" \
  "global_load_dwordx4 v[88:91], v155, %[h0] offset:3072\n\t" \
  "global_load_dwordx4 v[92:95], v155, %[h1] offset:3072\n\t" \
  "v_add_u32 v155, 4096, v155\n\t" \
  "s_add_u32 m0, %[lw], 0\n\t" \
  "s_nop 0\n\t" \
  "global_load_lds_dwordx4 v144, %[wd]\n\t" \
  "s_add_u32 m0, %[lw], 1024\n\t" \
  "s_nop 0\n\t" \
  "global_load_lds_dwordx4 v145, %[wd]\n\t" \
  "s_add_u32 m0, %[lw], 2048\n\t" \
  "s_nop 0\n\t" \
  "global_load_lds_dwordx4 v146, %[wd]\n\t" \
  "s_add_u32 m0, %[lw], 3072\n\t" \
  "s_nop 0\n\t" \
  "global_load_lds_dwordx4 v147, %[wd]\n\t" \
  "s_add_u32 m0, %[lw], 4096\n\t" \
  "s_nop 0\n\t" \
  "global_load_lds_dwordx4 v148, %[wd]\n\t" \
  "s_add_u32 m0, %[lw], 5120\n\t" \
  "s_nop 0\n\t" \
  "global_load_lds_dwordx4 v149, %[wd]\n\t" \
  "s_add_u32 m0, %[lw], 6144\n\t" \
  "s_nop 0\n\t" \
  "global_load_lds_dwordx4 v150, %[wd]\n\t" \
  "s_add_u32 m0, %[lw], 7168\n\t" \
  "s_nop 0\n\t" \
  "global_load_lds_dwordx4 v151, %[wd]\n\t" \
  "v_add_u32 v144, 0x10000, v144\n\t" \
  "v_add_u32 v145, 0x10000, v145\n\t" \
  "v_add_u32 v146, 0x10000, v146\n\t" \
  "v_add_u32 v147, 0x10000, v147\n\t" \
  "v_add_u32 v148, 0x10000, v148\n\t" \
  "v_add_u32 v149, 0x10000, v149\n\t" \
  "v_add_u32 v150, 0x10000, v150\n\t" \
  "v_add_u32 v151, 0x10000, v151\n\t" \
  "s_waitcnt vmcnt(16)\n\t" \
  "s_barrier\n\t" \
  "s_add_u32 m0, %[lk], 33792\n\t" \
  "s_nop 0\n\t" \
  "global_load_lds_dwordx4 v152, %[kg]\n\t" \
  "global_load_lds_dwordx4 v152, %[kg] offset:1024\n\t" \
  "s_add_u32 m0, %[lk], 41984\n\t" \
  "v_add_u32 v152, 8192, v152\n\t" \
  "global_load_lds_dwordx4 v153, %[vg]\n\t" \
  "global_load_lds_dwordx4 v153, %[vg] offset:1024\n\t" \
  "s_add_u32 m0, %[lt], 33792\n\t" \
  "s_mov_b64 exec, 0xff\n\t" \
  "global_load_lds_dwordx4 v154, %[vg]\n\t" \
  "s_mov_b64 exec, -1\n\t" \
  "v_add_u32 v153, 0x2200, v153\n\t" \
  "v_add_u32 v154, 0x2200, v154\n\t" \
  "s_setprio 0\n\t" \
  "ds_read_b64_tr_b16 v[128:129], %[r0] offset:8192\n\t" \
  "ds_read_b64_tr_b16 v[130:131], %[r0] offset:9216\n\t" \
  "ds_read_b64_tr_b16 v[132:133], %[r1] offset:8192\n\t" \
  "ds_read_b64_tr_b16 v[134:135], %[r1] offset:9216\n\t" \
  "ds_read_b64_tr_b16 v[136:137], %[r0] offset:10240\n\t" \
  "ds_read_b64_tr_b16 v[138:139], %[r0] offset:11264\n\t" \
  "s_waitcnt lgkmcnt(4)\n\t" \
  "v_mfma_f32_32x32x16_bf16 v[0:15], v[128:131], v[96:99], v[0:15]\n\t" \
  "v_mfma_f32_32x32x16_bf16 v[32:47], v[128:131], v[100:103], v[32:47]\n\t" \
  "ds_read_b64_tr_b16 v[140:141], %[r1] offset:10240\n\t" \
  "ds_read_b64_tr_b16 v[142:143], %[r1] offset:11264\n\t" \
  "s_waitcnt lgkmcnt(4)\n\t" \
  "v_mfma_f32_32x32x16_bf16 v[16:31], v[132:135], v[96:99], v[16:31]\n\t" \
  "v_mfma_f32_32x32x16_bf16 v[48:63], v[132:135], v[100:103], v[48:63]\n\t" \
  "ds_read_b64_tr_b16 v[128:129], %[r0] offset:12288\n\t" \
  "ds_read_b64_tr_b16 v[130:131], %[r0] offset:13312\n\t" \
  "s_waitcnt lgkmcnt(4)\n\t" \
  "v_mfma_f32_32x32x16_bf16 v[0:15], v[136:139], v[104:107], v[0:15]\n\t" \
  "v_mfma_f32_32x32x16_bf16 v[32:47], v[136:139], v[108:111], v[32:47]\n\t" \
  "ds_read_b64_tr_b16 v[132:133], %[r1] offset:12288\n\t" \
  "ds_read_b64_tr_b16 v[134:135], %[r1] offset:13312\n\t" \
  "s_waitcnt lgkmcnt(4)\n\t" \
  "v_mfma_f32_32x32x16_bf16 v[16:31], v[140:143], v[104:107], v[16:31]\n\t" \
  "v_mfma_f32_32x32x16_bf16 v[48:63], v[140:143], v[108:111], v[48:63]\n\t" \
  "ds_read_b64_tr_b16 v[136:137], %[r0] offset:14336\n\t" \
  "ds_read_b64_tr_b16 v[138:139], %[r0] offset:15360\n\t" \
  "s_waitcnt lgkmcnt(4)\n\t" \
  "v_mfma_f32_32x32x16_bf16 v[0:15], v[128:131], v[112:115], v[0:15]\n\t" \
  "v_mfma_f32_32x32x16_bf16 v[32:47], v[128:131], v[116:119], v[32:47]\n\t" \
  "ds_read_b64_tr_b16 v[140:141], %[r1] offset:14336\n\t" \
  "ds_read_b64_tr_b16 v[142:143], %[r1] offset:15360\n\t" \
  "s_waitcnt lgkmcnt(4)\n\t" \
  "v_mfma_f32_32x32x16_bf16 v[16:31], v[132:135], v[112:115], v[16:31]\n\t" \
  "v_mfma_f32_32x32x16_bf16 v[48:63], v[132:135], v[116:119], v[48:63]\n\t" \
  "s_waitcnt lgkmcnt(2)\n\t" \
  "v_mfma_f32_32x32x16_bf16 v[0:15], v[136:139], v[120:123], v[0:15]\n\t" \
  "v_mfma_f32_32x32x16_bf16 v[32:47], v[136:139], v[124:127], v[32:47]\n\t" \
  "s_waitcnt lgkmcnt(0)\n\t" \
  "v_mfma_f32_32x32x16_bf16 v[16:31], v[140:143], v[120:123], v[16:31]\n\t" \
  "v_mfma_f32_32x32x16_bf16 v[48:63], v[140:143], v[124:127], v[48:63]\n\t" \
  "s_setprio 3\n\t" \
  "global_load_dwordx4 v[96:99], v155, %[h0] offset:0\n\t" \
  "global_load_dwordx4 v[100:103], v155, %[h1] offset:0\n\t" \
  "global_load_dwordx4 v[104:107], v155, %[h0] offset:1024\n\t" \
  "global_load_dwordx4 v[108:111], v155, %[h1] offset:1024\n\t" \
  "global_load_dwordx4 v[112:115], v155, %[h0] offset:2048\n\t" \
  "global_load_dwordx4 v[116:119], v155, %[h1] offset:2048\n\t" \
  "global_load_dwordx4 v[120:123], v155, %[h0] offset:3072\n\t" \
  "global_load_dwordx4 v[124:127], v155, %[h1] offset:3072\n\t" \
  "v_add_u32 v155, 4096, v155\n\t" \
  "s_add_u32 m0, %[lw], 8192\n\t" \
  "s_nop 0\n\t" \
  "global_load_lds_dwordx4 v144, %[wd]\n\t" \
  "s_add_u32 m0, %[lw], 9216\n\t" \
  "s_nop 0\n\t" \
  "global_load_lds_dwordx4 v145, %[wd]\n\t" \
  "s_add_u32 m0, %[lw], 10240\n\t" \
  "s_nop 0\n\t" \
  "global_load_lds_dwordx4 v146, %[wd]\n\t" \
  "s_add_u32 m0, %[lw], 11264\n\t" \
  "s_nop 0\n\t" \
  "global_load_lds_dwordx4 v147, %[wd]\n\t" \
  "s_add_u32 m0, %[lw], 12288\n\t" \
  "s_nop 0\n\t" \
  "global_load_lds_dwordx4 v148, %[wd]\n\t" \
  "s_add_u32 m0, %[lw], 13312\n\t" \
  "s_nop 0\n\t" \
  "global_load_lds_dwordx4 v149, %[wd]\n\t" \
  "s_add_u32 m0, %[lw], 14336\n\t" \
  "s_nop 0\n\t" \
  "global_load_lds_dwordx4 v150, %[wd]\n\t" \
  "s_add_u32 m0, %[lw], 15360\n\t" \
  "s_nop 0\n\t" \
  "global_load_lds_dwordx4 v151, %[wd]\n\t" \
  "v_add_u32 v144, 0x10000, v144\n\t" \
  "v_add_u32 v145, 0x10000, v145\n\t" \
  "v_add_u32 v146, 0x10000, v146\n\t" \
  "v_add_u32 v147, 0x10000, v147\n\t" \
  "v_add_u32 v148, 0x10000, v148\n\t" \
  "v_add_u32 v149, 0x10000, v149\n\t" \
  "v_add_u32 v150, 0x10000, v150\n\t" \
  "v_add_u32 v151, 0x10000, v151\n\t" \
  "s_waitcnt vmcnt(16)\n\t" \
  "s_barrier\n\t" \
  "s_add_u32 m0, %[lk], 0\n\t" \
  "s_nop 0\n\t" \
  "global_load_lds_dwordx4 v152, %[kg]\n\t" \
  "global_load_lds_dwordx4 v152, %[kg] offset:1024\n\t" \
  "s_add_u32 m0, %[lk], 8192\n\t" \
  "v_add_u32 v152, 8192, v152\n\t" \
  "global_load_lds_dwordx4 v153, %[vg]\n\t" \
  "global_load_lds_dwordx4 v153, %[vg] offset:1024\n\t" \
  "s_add_u32 m0, %[lt], 0\n\t" \
  "s_mov_b64 exec, 0xff\n\t" \
  "global_load_lds_dwordx4 v154, %[vg]\n\t" \
  "s_mov_b64 exec, -1\n\t" \
  "v_add_u32 v153, 0x2200, v153\n\t" \
  "v_add_u32 v154, 0x2200, v154\n\t" \
  "s_setprio 0\n\t" \
  "ds_read_b64_tr_b16 v[128:129], %[r0] offset:0\n\t" \
  "ds_read_b64_tr_b16 v[130:131], %[r0] offset:1024\n\t" \
  "ds_read_b64_tr_b16 v[132:133], %[r1] offset:0\n\t" \
  "ds_read_b64_tr_b16 v[134:135], %[r1] offset:1024\n\t" \
  "ds_read_b64_tr_b16 v[136:137], %[r0] offset:2048\n\t" \
  "ds_read_b64_tr_b16 v[138:139], %[r0] offset:3072\n\t" \
  "s_waitcnt lgkmcnt(4)\n\t" \
  "v_mfma_f32_32x32x16_bf16 v[0:15], v[128:131], v[64:67], v[0:15]\n\t" \
  "v_mfma_f32_32x32x16_bf16 v[32:47], v[128:131], v[68:71], v[32:47]\n\t" \
  "ds_read_b64_tr_b16 v[140:141], %[r1] offset:2048\n\t" \
  "ds_read_b64_tr_b16 v[142:143], %[r1] offset:3072\n\t" \
  "s_waitcnt lgkmcnt(4)\n\t" \
  "v_mfma_f32_32x32x16_bf16 v[16:31], v[132:135], v[64:67], v[16:31]\n\t" \
  "v_mfma_f32_32x32x16_bf16 v[48:63], v[132:135], v[68:71], v[48:63]\n\t" \
  "ds_read_b64_tr_b16 v[128:129], %[r0] offset:4096\n\t" \
  "ds_read_b64_tr_b16 v[130:131], %[r0] offset:5120\n\t" \
  "s_waitcnt lgkmcnt(4)\n\t" \
  "v_mfma_f32_32x32x16_bf16 v[0:15], v[136:139], v[72:75], v[0:15]\n\t" \
  "v_mfma_f32_32x32x16_bf16 v[32:47], v[136:139], v[76:79], v[32:47]\n\t" \
  "ds_read_b64_tr_b16 v[132:133], %[r1] offset:4096\n\t" \
  "ds_read_b64_tr_b16 v[134:135], %[r1] offset:5120\n\t" \
  "s_waitcnt lgkmcnt(4)\n\t" \
  "v_mfma_f32_32x32x16_bf16 v[16:31], v[140:143], v[72:75], v[16:31]\n\t" \
  "v_mfma_f32_32x32x16_bf16 v[48:63], v[140:143], v[76:79], v[48:63]\n\t" \
  "ds_read_b64_tr_b16 v[136:137], %[r0] offset:6144\n\t" \
  "ds_read_b64_tr_b16 v[138:139], %[r0] offset:7168\n\t" \
  "s_waitcnt lgkmcnt(4)\n\t" \
  "v_mfma_f32_32x32x16_bf16 v[0:15], v[128:131], v[80:83], v[0:15]\n\t" \
  "v_mfma_f32_32x32x16_bf16 v[32:47], v[128:131], v[84:87], v[32:47]\n\t" \
  "ds_read_b64_tr_b16 v[140:141], %[r1] offset:6144\n\t" \
  "ds_read_b64_tr_b16 v[142:143], %[r1] offset:7168\n\t" \
  "s_waitcnt lgkmcnt(4)\n\t" \
  "v_mfma_f32_32x32x16_bf16 v[16:31], v[132:135], v[80:83], v[16:31]\n\t" \
  "v_mfma_f32_32x32x16_bf16 v[48:63], v[132:135], v[84:87], v[48:63]\n\t" \
  "s_waitcnt lgkmcnt(2)\n\t" \
  "v_mfma_f32_32x32x16_bf16 v[0:15], v[136:139], v[88:91], v[0:15]\n\t" \
  "v_mfma_f32_32x32x16_bf16 v[32:47], v[136:139], v[92:95], v[32:47]\n\t" \
  "s_waitcnt lgkmcnt(0)\n\t" \
  "v_mfma_f32_32x32x16_bf16 v[16:31], v[140:143], v[88:91], v[16:31]\n\t" \
  "v_mfma_f32_32x32x16_bf16 v[48:63], v[140:143], v[92:95], v[48:63]\n\t" \
  "s_setprio 3\n\t" \
  "global_load_dwordx4 v[64:67], v155, %[h0] offset:0\n\t" \
  "global_load_dwordx4 v[68:71], v155, %[h1] offset:0\n\t" \
  "global_load_dwordx4 v[72:75], v155, %[h0] offset:1024\n\t" \
  "global_load_dwordx4 v[76:79], v155, %[h1] offset:1024\n\t" \
  "global_load_dwordx4 v[80:83], v155, %[h0] offset:2048\n\t" \
  "global_load_dwordx4 v[84:87], v155, %[h1] offset:2048\n\t" \
  "global_load_dwordx4 v[88:91], v155, %[h0] offset:3072\n\t" \
  "global_load_dwordx4 v[92:95], v155, %[h1] offset:3072\n\t" \
  "v_add_u32 v155, 4096, v155\n\t" \
  "s_add_u32 m0, %[lw], 0\n\t" \
  "s_nop 0\n\t" \
  "global_load_lds_dwordx4 v144, %[wd]\n\t" \
  "s_add_u32 m0, %[lw], 1024\n\t" \
  "s_nop 0\n\t" \
  "global_load_lds_dwordx4 v145, %[wd]\n\t" \
  "s_add_u32 m0, %[lw], 2048\n\t" \
  "s_nop 0\n\t" \
  "global_load_lds_dwordx4 v146, %[wd]\n\t" \
  "s_add_u32 m0, %[lw], 3072\n\t" \
  "s_nop 0\n\t" \
  "global_load_lds_dwordx4 v147, %[wd]\n\t" \
  "s_add_u32 m0, %[lw], 4096\n\t" \
  "s_nop 0\n\t" \
  "global_load_lds_dwordx4 v148, %[wd]\n\t" \
  "s_add_u32 m0, %[lw], 5120\n\t" \
  "s_nop 0\n\t" \
  "global_load_lds_dwordx4 v149, %[wd]\n\t" \
  "s_add_u32 m0, %[lw], 6144\n\t" \
  "s_nop 0\n\t" \
  "global_load_lds_dwordx4 v150, %[wd]\n\t" \
  "s_add_u32 m0, %[lw], 7168\n\t" \
  "s_nop 0\n\t" \
  "global_load_lds_dwordx4 v151, %[wd]\n\t" \
  "v_add_u32 v144, 0x10000, v144\n\t" \
  "v_add_u32 v145, 0x10000, v145\n\t" \
  "v_add_u32 v146, 0x10000, v146\n\t" \
  "v_add_u32 v147, 0x10000, v147\n\t" \
  "v_add_u32 v148, 0x10000, v148\n\t" \
  "v_add_u32 v149, 0x10000, v149\n\t" \
  "v_add_u32 v150, 0x10000, v150\n\t" \
  "v_add_u32 v151, 0x10000, v151\n\t" \
  "s_waitcnt vmcnt(16)\n\t" \
  "s_barrier\n\t" \
  "s_add_u32 m0, %[lk], 16896\n\t" \
  "s_nop 0\n\t" \
  "global_load_lds_dwordx4 v152, %[kg]\n\t" \
  "global_load_lds_dwordx4 v152, %[kg] offset:1024\n\t" \
  "s_add_u32 m0, %[lk], 25088\n\t" \
  "v_add_u32 v152, 8192, v152\n\t" \
  "global_load_lds_dwordx4 v153, %[vg]\n\t" \
  "global_load_lds_dwordx4 v153, %[vg] offset:1024\n\t" \
  "s_add_u32 m0, %[lt], 16896\n\t" \
  "s_mov_b64 exec, 0xff\n\t" \
  "global_load_lds_dwordx4 v154, %[vg]\n\t" \
  "s_mov_b64 exec, -1\n\t" \
  "v_add_u32 v153, 0x2200, v153\n\t" \
  "v_add_u32 v154, 0x2200, v154\n\t" \
  "v_add_u32 v156, 0, %[wt]\n\t" \
  "s_add_u32 m0, %[lq], 116736\n\t" \
  "s_nop 0\n\t" \
  "global_load_lds_dwordx4 v156, %[wo]\n\t" \
  "v_add_u32 v156, 8192, %[wt]\n\t" \
  "s_add_u32 m0, %[lq], 117760\n\t" \
  "s_nop 0\n\t" \
  "global_load_lds_dwordx4 v156, %[wo]\n\t" \
  "v_add_u32 v156, 16384, %[wt]\n\t" \
  "s_add_u32 m0, %[lq], 118784\n\t" \
  "s_nop 0\n\t" \
  "global_load_lds_dwordx4 v156, %[wo]\n\t" \
  "v_add_u32 v156, 24576, %[wt]\n\t" \
  "s_add_u32 m0, %[lq], 119808\n\t" \
  "s_nop 0\n\t" \
  "global_load_lds_dwordx4 v156, %[wo]\n\t" \
  "v_add_u32 v156, 131072, %[wt]\n\t" \
  "s_add_u32 m0, %[lq], 133120\n\t" \
  "s_nop 0\n\t" \
  "global_load_lds_dwordx4 v156, %[wo]\n\t" \
  "v_add_u32 v156, 139264, %[wt]\n\t" \
  "s_add_u32 m0, %[lq], 134144\n\t" \
  "s_nop 0\n\t" \
  "global_load_lds_dwordx4 v156, %[wo]\n\t" \
  "v_add_u32 v156, 147456, %[wt]\n\t" \
  "s_add_u32 m0, %[lq], 135168\n\t" \
  "s_nop 0\n\t" \
  "global_load_lds_dwordx4 v156, %[wo]\n\t" \
  "v_add_u32 v156, 155648, %[wt]\n\t" \
  "s_add_u32 m0, %[lq], 136192\n\t" \
  "s_nop 0\n\t" \
  "global_load_lds_dwordx4 v156, %[wo]\n\t" \
  "s_mov_b32 m0, %[lx]\n\t" \
  "v_xor_b32 v156, 64, %[dx]\n\t" \
  "global_load_lds_dwordx4 %[dx], %[xg]\n\t" \
  "s_add_u32 m0, %[lx], 1024\n\t" \
  "v_add_u32 v156, 8192, v156\n\t" \
  "global_load_lds_dwordx4 v156, %[xg]\n\t" \
  "s_setprio 0\n\t" \
  "ds_read_b64_tr_b16 v[128:129], %[r0] offset:8192\n\t" \
  "ds_read_b64_tr_b16 v[130:131], %[r0] offset:9216\n\t" \
  "ds_read_b64_tr_b16 v[132:133], %[r1] offset:8192\n\t" \
  "ds_read_b64_tr_b16 v[134:135], %[r1] offset:9216\n\t" \
  "ds_read_b64_tr_b16 v[136:137], %[r0] offset:10240\n\t" \
  "ds_read_b64_tr_b16 v[138:139], %[r0] offset:11264\n\t" \
  "s_waitcnt lgkmcnt(4)\n\t" \
  "v_mfma_f32_32x32x16_bf16 v[0:15], v[128:131], v[96:99], v[0:15]\n\t" \
  "v_mfma_f32_32x32x16_bf16 v[32:47], v[128:131], v[100:103], v[32:47]\n\t" \
  "ds_read_b64_tr_b16 v[140:141], %[r1] offset:10240\n\t" \
  "ds_read_b64_tr_b16 v[142:143], %[r1] offset:11264\n\t" \
  "s_waitcnt lgkmcnt(4)\n\t" \
  "v_mfma_f32_32x32x16_bf16 v[16:31], v[132:135], v[96:99], v[16:31]\n\t" \
  "v_mfma_f32_32x32x16_bf16 v[48:63], v[132:135], v[100:103], v[48:63]\n\t" \
  "ds_read_b64_tr_b16 v[128:129], %[r0] offset:12288\n\t" \
  "ds_read_b64_tr_b16 v[130:131], %[r0] offset:13312\n\t" \
  "s_waitcnt lgkmcnt(4)\n\t" \
  "v_mfma_f32_32x32x16_bf16 v[0:15], v[136:139], v[104:107], v[0:15]\n\t" \
  "v_mfma_f32_32x32x16_bf16 v[32:47], v[136:139], v[108:111], v[32:47]\n\t" \
  "ds_read_b64_tr_b16 v[132:133], %[r1] offset:12288\n\t" \
  "ds_read_b64_tr_b16 v[134:135], %[r1] offset:13312\n\t" \
  "s_waitcnt lgkmcnt(4)\n\t" \
  "v_mfma_f32_32x32x16_bf16 v[16:31], v[140:143], v[104:107], v[16:31]\n\t" \
  "v_mfma_f32_32x32x16_bf16 v[48:63], v[140:143], v[108:111], v[48:63]\n\t" \
  "ds_read_b64_tr_b16 v[136:137], %[r0] offset:14336\n\t" \
  "ds_read_b64_tr_b16 v[138:139], %[r0] offset:15360\n\t" \
  "s_waitcnt lgkmcnt(4)\n\t" \
  "v_mfma_f32_32x32x16_bf16 v[0:15], v[128:131], v[112:115], v[0:15]\n\t" \
  "v_mfma_f32_32x32x16_bf16 v[32:47], v[128:131], v[116:119], v[32:47]\n\t" \
  "ds_read_b64_tr_b16 v[140:141], %[r1] offset:14336\n\t" \
  "ds_read_b64_tr_b16 v[142:143], %[r1] offset:15360\n\t" \
  "s_waitcnt lgkmcnt(4)\n\t" \
  "v_mfma_f32_32x32x16_bf16 v[16:31], v[132:135], v[112:115], v[16:31]\n\t" \
  "v_mfma_f32_32x32x16_bf16 v[48:63], v[132:135], v[116:119], v[48:63]\n\t" \
  "s_waitcnt lgkmcnt(2)\n\t" \
  "v_mfma_f32_32x32x16_bf16 v[0:15], v[136:139], v[120:123], v[0:15]\n\t" \
  "v_mfma_f32_32x32x16_bf16 v[32:47], v[136:139], v[124:127], v[32:47]\n\t" \
  "s_waitcnt lgkmcnt(0)\n\t" \
  "v_mfma_f32_32x32x16_bf16 v[16:31], v[140:143], v[120:123], v[16:31]\n\t" \
  "v_mfma_f32_32x32x16_bf16 v[48:63], v[140:143], v[124:127], v[48:63]\n\t" \
  "s_setprio 3\n\t" \
  "s_waitcnt vmcnt(10)\n\t" \
  "s_barrier\n\t" \
  "v_add_u32 v156, 262144, %[wt]\n\t" \
  "s_add_u32 m0, %[lq], 33792\n\t" \
  "s_nop 0\n\t" \
  "global_load_lds_dwordx4 v156, %[wo]\n\t" \
  "v_add_u32 v156, 270336, %[wt]\n\t" \
  "s_add_u32 m0, %[lq], 34816\n\t" \
  "s_nop 0\n\t" \
  "global_load_lds_dwordx4 v156, %[wo]\n\t" \
  "v_add_u32 v156, 278528, %[wt]\n\t" \
  "s_add_u32 m0, %[lq], 35840\n\t" \
  "s_nop 0\n\t" \
  "global_load_lds_dwordx4 v156, %[wo]\n\t" \
  "v_add_u32 v156, 286720, %[wt]\n\t" \
  "s_add_u32 m0, %[lq], 36864\n\t" \
  "s_nop 0\n\t" \
  "global_load_lds_dwordx4 v156, %[wo]\n\t" \
  "v_add_u32 v156, 393216, %[wt]\n\t" \
  "s_add_u32 m0, %[lq], 0\n\t" \
  "s_nop 0\n\t" \
  "global_load_lds_dwordx4 v156, %[wo]\n\t" \
  "v_add_u32 v156, 401408, %[wt]\n\t" \
  "s_add_u32 m0, %[lq], 1024\n\t" \
  "s_nop 0\n\t" \
  "global_load_lds_dwordx4 v156, %[wo]\n\t" \
  "v_add_u32 v156, 409600, %[wt]\n\t" \
  "s_add_u32 m0, %[lq], 2048\n\t" \
  "s_nop 0\n\t" \
  "global_load_lds_dwordx4 v156, %[wo]\n\t" \
  "v_add_u32 v156, 417792, %[wt]\n\t" \
  "s_mov_b32 m0, %[l3]\n\t" \
  "s_nop 0\n\t" \
  "global_load_lds_dwordx4 v156, %[wo]\n\t" \
  "s_setprio 0\n\t" \
  "ds_read_b64_tr_b16 v[128:129], %[r0] offset:0\n\t" \
  "ds_read_b64_tr_b16 v[130:131], %[r0] offset:1024\n\t" \
  "ds_read_b64_tr_b16 v[132:133], %[r1] offset:0\n\t" \
  "ds_read_b64_tr_b16 v[134:135], %[r1] offset:1024\n\t" \
  "ds_read_b64_tr_b16 v[136:137], %[r0] offset:2048\n\t" \
  "ds_read_b64_tr_b16 v[138:139], %[r0] offset:3072\n\t" \
  "s_waitcnt lgkmcnt(4)\n\t" \
  "v_mfma_f32_32x32x16_bf16 v[0:15], v[128:131], v[64:67], v[0:15]\n\t" \
  "v_mfma_f32_32x32x16_bf16 v[32:47], v[128:131], v[68:71], v[32:47]\n\t" \
  "ds_read_b64_tr_b16 v[140:141], %[r1] offset:2048\n\t" \
  "ds_read_b64_tr_b16 v[142:143], %[r1] offset:3072\n\t" \
  "s_waitcnt lgkmcnt(4)\n\t" \
  "v_mfma_f32_32x32x16_bf16 v[16:31], v[132:135], v[64:67], v[16:31]\n\t" \
  "v_mfma_f32_32x32x16_bf16 v[48:63], v[132:135], v[68:71], v[48:63]\n\t" \
  "ds_read_b64_tr_b16 v[128:129], %[r0] offset:4096\n\t" \
  "ds_read_b64_tr_b16 v[130:131], %[r0] offset:5120\n\t" \
  "s_waitcnt lgkmcnt(4)\n\t" \
  "v_mfma_f32_32x32x16_bf16 v[0:15], v[136:139], v[72:75], v[0:15]\n\t" \
  "v_mfma_f32_32x32x16_bf16 v[32:47], v[136:139], v[76:79], v[32:47]\n\t" \
  "ds_read_b64_tr_b16 v[132:133], %[r1] offset:4096\n\t" \
  "ds_read_b64_tr_b16 v[134:135], %[r1] offset:5120\n\t" \
  "s_waitcnt lgkmcnt(4)\n\t" \
  "v_mfma_f32_32x32x16_bf16 v[16:31], v[140:143], v[72:75], v[16:31]\n\t" \
  "v_mfma_f32_32x32x16_bf16 v[48:63], v[140:143], v[76:79], v[48:63]\n\t" \
  "ds_read_b64_tr_b16 v[136:137], %[r0] offset:6144\n\t" \
  "ds_read_b64_tr_b16 v[138:139], %[r0] offset:7168\n\t" \
  "s_waitcnt lgkmcnt(4)\n\t" \
  "v_mfma_f32_32x32x16_bf16 v[0:15], v[128:131], v[80:83], v[0:15]\n\t" \
  "v_mfma_f32_32x32x16_bf16 v[32:47], v[128:131], v[84:87], v[32:47]\n\t" \
  "ds_read_b64_tr_b16 v[140:141], %[r1] offset:6144\n\t" \
  "ds_read_b64_tr_b16 v[142:143], %[r1] offset:7168\n\t" \
  "s_waitcnt lgkmcnt(4)\n\t" \
  "v_mfma_f32_32x32x16_bf16 v[16:31], v[132:135], v[80:83], v[16:31]\n\t" \
  "v_mfma_f32_32x32x16_bf16 v[48:63], v[132:135], v[84:87], v[48:63]\n\t" \
  "s_waitcnt lgkmcnt(2)\n\t" \
  "v_mfma_f32_32x32x16_bf16 v[0:15], v[136:139], v[88:91], v[0:15]\n\t" \
  "v_mfma_f32_32x32x16_bf16 v[32:47], v[136:139], v[92:95], v[32:47]\n\t" \
  "s_waitcnt lgkmcnt(0)\n\t" \
  "v_mfma_f32_32x32x16_bf16 v[16:31], v[140:143], v[88:91], v[16:31]\n\t" \
  "v_mfma_f32_32x32x16_bf16 v[48:63], v[140:143], v[92:95], v[48:63]\n\t" \
  "s_setprio 3\n\t" \
  "s_nop 15\n\t" \
  "s_nop 3\n\t" \
  "ds_write_b128 %[pw], v[0:3] offset:0\n\t" \
  "s_sleep 1\n\t" \
  "ds_write_b128 %[pw], v[4:7] offset:1024\n\t" \
  "s_sleep 1\n\t" \
  "ds_write_b128 %[pw], v[8:11] offset:2048\n\t" \
  "s_sleep 1\n\t" \
  "ds_write_b128 %[pw], v[12:15] offset:3072\n\t" \
  "s_sleep 1\n\t" \
  "ds_write_b128 %[pw], v[16:19] offset:4096\n\t" \
  "s_sleep 1\n\t" \
  "ds_write_b128 %[pw], v[20:23] offset:5120\n\t" \
  "s_sleep 1\n\t" \
  "ds_write_b128 %[pw], v[24:27] offset:6144\n\t" \
  "s_sleep 1\n\t" \
  "ds_write_b128 %[pw], v[28:31] offset:7168\n\t" \
  "s_sleep 1\n\t" \
  "ds_write_b128 %[pw], v[32:35] offset:8192\n\t" \
  "s_sleep 1\n\t" \
  "ds_write_b128 %[pw], v[36:39] offset:9216\n\t" \
  "s_sleep 1\n\t" \
  "ds_write_b128 %[pw], v[40:43] offset:10240\n\t" \
  "s_sleep 1\n\t" \
  "ds_write_b128 %[pw], v[44:47] offset:11264\n\t" \
  "s_sleep 1\n\t" \
  "ds_write_b128 %[pw], v[48:51] offset:12288\n\t" \
  "s_sleep 1\n\t" \
  "ds_write_b128 %[pw], v[52:55] offset:13312\n\t" \
  "s_sleep 1\n\t" \
  "ds_write_b128 %[pw], v[56:59] offset:14336\n\t" \
  "s_sleep 1\n\t" \
  "ds_write_b128 %[pw], v[60:63] offset:15360\n\t" \
  "s_sleep 1\n\t" \
  "s_waitcnt vmcnt(0) lgkmcnt(0)\n\t" \
  "s_barrier\n\t"
#define AUX_CLOBBER "v0", "v1", "v2", "v3", "v4", "v5", "v6", "v7", "v8", "v9", "v10", "v11", "v12", "v13", "v14", "v15", "v16", "v17", "v18", "v19", "v20", "v21", "v22", "v23", "v24", "v25", "v26", "v27", "v28", "v29", "v30", "v31", "v32", "v33", "v34", "v35", "v36", "v37", "v38", "v39", "v40", "v41", "v42", "v43", "v44", "v45", "v46", "v47", "v48", "v49", "v50", "v51", "v52", "v53", "v54", "v55", "v56", "v57", "v58", "v59", "v60", "v61", "v62", "v63", "v64", "v65", "v66", "v67", "v68", "v69", "v70", "v71", "v72", "v73", "v74", "v75", "v76", "v77", "v78", "v79", "v80", "v81", "v82", "v83", "v84", "v85", "v86", "v87", "v88", "v89", "v90", "v91", "v92", "v93", "v94", "v95", "v96", "v97", "v98", "v99", "v100", "v101", "v102", "v103", "v104", "v105", "v106", "v107", "v108", "v109", "v110", "v111", "v112", "v113", "v114", "v115", "v116", "v117", "v118", "v119", "v120", "v121", "v122", "v123", "v124", "v125", "v126", "v127", "v128", "v129", "v130", "v131", "v132", "v133", "v134", "v135", "v136", "v137", "v138", "v139", "v140", "v141", "v142", "v143", "v144", "v145", "v146", "v147", "v148", "v149", "v150", "v151", "v156"
#define AUX_BT
#define AT_PM , [em] "s"(0x0011FFFF0011FFFFull)

template <int N> struct IntC { static constexpr int value = N; };
constexpr int AT_K = 0, AT_V = 8192, AT_SLOT = 8192 + 8704, AT_NS = 3;
constexpr int AT_W = 51200;
constexpr int AT_WO = AT_W + 65536;
constexpr int AT_WL = AT_W - 1024;
constexpr int AT_X = AT_WO + 32768;
constexpr int AT_LDS = AT_X + 8192;

DEV void at_tile(bf16x8 kf, bf16x8 v0, bf16x8 v1, bf16x8 qf, const f32x16& negm, f32x16& o0, f32x16& o1) {
  const f32x16 s = mfma(kf, qf, negm);
  float p[16];
#pragma unroll
  for (int r = 0; r < 16; ++r) p[r] = __builtin_amdgcn_exp2f(s[r]);
  o0 = mfma(v0, pack8(p[0], p[1], p[2], p[3], p[4], p[5], p[6], p[7]), o0);
  o1 = mfma(v1, pack8(p[8], p[9], p[10], p[11], p[12], p[13], p[14], p[15]), o1);
}

DEV void at_true_max(const char* kg, const char* vg, bf16x8 qf, int lane, f32x16& o_out) {
  const int l31 = lane & 31, hi = lane >> 5;
  f32x16 zero, o0, o1;
#pragma unroll
  for (int r = 0; r < 16; ++r) zero[r] = 0.f;
  float m = -INFINITY;
  for (int t = 0; t < NTB; ++t) {
    const f32x16 s = mfma(*(const bf16x8*)(kg + t * 1024 + lane * 16), qf, zero);
#pragma unroll
    for (int r = 0; r < 16; ++r) m = fmaxf(m, s[r]);
  }
  m = swap_max(m);
  f32x16 negm;
#pragma unroll
  for (int r = 0; r < 16; ++r) negm[r] = -m;
  o0 = zero;
  o1 = zero;
  const int vo = hi * 272 + (l31 < 16 ? 16 + 16 * l31 : 0);
  for (int t = 0; t < NTB; ++t) {
    at_tile(*(const bf16x8*)(kg + t * 1024 + lane * 16), *(const bf16x8*)(vg + t * VT + vo),
            *(const bf16x8*)(vg + t * VT + 544 + vo), qf, negm, o0, o1);
  }
  o_out = o0 + o1;
}

DEV void at_aux(lds_ptr smem, const char* x, const char* wo, const char* wd, char* ws, const char* kg, const char* vg, int j, int lane) {
  const int xcd = blockIdx.x & 7, loc = blockIdx.x >> 3;
  const int tp = xcd * 4 + (loc & 3), cblk = loc >> 2;
  const int hi = lane >> 5;
  const uint32_t lbase = lds_addr(smem);
  const int drow = lane >> 3;
  const uint32_t dw = drow * 1024 + (((lane & 7) ^ (((drow >> 1) & 1) << 2)) << 4);
  const int g16 = (lane >> 4) & 1, q4 = (lane >> 2) & 3, p4 = lane & 3, row = 4 * hi + q4;
  const uint32_t lw = lbase + AT_W + j * 16384;
  const uint32_t r0 = lw + row * 128 + (((row >> 1) & 1) << 6) + 32 * g16 + 8 * p4;
  const uint32_t r1 = r0 ^ 64;
  const uint32_t pw = lw + lane * 16;
  uint32_t vk = 8192 + j * 2048 + lane * 16;
  uint32_t vv = 8 * VT + j * 2048 + lane * 16;
  uint32_t vt = 8 * VT + 8192 + j * 128 + lane * 16;
  uint32_t vh = lane * 16;
  const char* h0 = ws + WS_H + ((size_t)(tp * 2) * NKG + j * 28) * 1024;
  const char* h1 = h0 + NKG * 1024;
  const char* wj = wd + (size_t)j * 448 * 1024 + cblk * 128;
  const uint32_t wt = dw + j * 4 * 8192;
  const uint32_t dx = drow * 1024 + (((lane & 7) ^ (drow >> 1)) << 4);
  asm volatile(AT_AUX_ASM
               : "+{v152}"(vk), "+{v153}"(vv), "+{v154}"(vt), "+{v155}"(vh)
               : [kg] "s"(kg), [vg] "s"(vg), [wd] "s"(wj), [h0] "s"(h0), [h1] "s"(h1), [lk] "s"(lbase + j * 2048),
                 [lt] "s"(lbase + AT_V + 8192 + j * 128), [lw] "s"(lw), [dw] "v"(dw), [r0] "v"(r0), [r1] "v"(r1), [pw] "v"(pw), [wt] "v"(wt),
                 [wo] "s"(wo + cblk * 128), [lq] "s"(lbase + j * 4096),
                 [l3] "s"(lbase + (j == 3 ? AT_WL : j * 4096 + 3072)), [lx] "s"(lbase + AT_X + j * 2048), [dx] "v"(dx),
                 [xg] "s"(x + ((size_t)(tp * 64 + j * 16) * DM + cblk * 64) * 2) AUX_BT
               : "memory", "scc", AUX_CLOBBER);
}

__global__ __launch_bounds__(768, 3) void k_attn(const char* x, const char* wo, const char* wd, char* ws, char* out) {
  __shared__ __attribute__((aligned(16))) char smem_raw[AT_LDS];
  lds_ptr smem = (lds_ptr)smem_raw;
  const int lane = threadIdx.x & 63, w = uniform(threadIdx.x >> 6);
  const int xcd = blockIdx.x & 7, loc = blockIdx.x >> 3;
  const int head = xcd * 4 + (loc & 3), qb = loc >> 2;
  const int l31 = lane & 31, hi = lane >> 5;
  const char* kg = ws + WS_K + (size_t)head * NTB * 1024;
  const char* vg = ws + WS_V + (size_t)head * NTB * VT;
  const uint32_t lbase = lds_addr(smem);
  if (w >= 8) {
    at_aux(smem, x, wo, wd, ws, kg, vg, w - 8, lane);
    return;
  }
  {
    const uint32_t dv = w * 1024 + lane * 16, dq = 8192 + w * 64 + lane * 16;
    glds16(dv, kg, lbase + AT_K + w * 1024);
    glds16(dv, vg, lbase + AT_V + w * 1024);
    glds16_q(dq, vg, lbase + AT_V + 8192 + w * 64);
  }
  const int tq = qb * 8 + w;
  const int ko = AT_K + lane * 16;
  const int vo = AT_V + hi * 272 + (l31 < 16 ? 16 + 16 * l31 : 0);
  f32x16 o0, o1, negm, sc, sb_;
  bf16x8 qf, kn, kb_, va, vb, vc, vd;
  asm volatile(AT_PRO_ASM
               : "=&{v[0:15]}"(negm), "=&{v[16:31]}"(o0), "=&{v[32:47]}"(o1), "=&{v[48:63]}"(sc), "=&{v[64:79]}"(sb_),
                 "=&{v[80:83]}"(qf), "=&{v[84:87]}"(kb_), "=&{v[88:91]}"(kn), "=&{v[92:95]}"(va), "=&{v[96:99]}"(vb),
                 "=&{v[100:103]}"(vc), "=&{v[104:107]}"(vd)
               : [qo] "v"(lane * 16), [qb] "s"(ws + WS_Q + ((size_t)head * NTB + tq) * 1024), [ka] "v"(lbase + ko), [va] "v"(lbase + vo) AT_PM
               : "memory");
  const int tp = xcd * 4 + (loc & 3), cblk = loc >> 2;
  const int tb = w & 1, kq = w >> 1, fcb = kq >> 1, g0 = 2 * (kq & 1);
  char* orow = out + ((size_t)((tp * 2 + tb) * 32 + l31) * DM + cblk * 64 + fcb * 32 + 8 * g0 + 4 * hi) * 2;
  int slot = 0;
  for (int c = 0; c < 8; ++c) {
    const int nslot = slot == 2 ? 0 : slot + 1;
    const uint32_t ka = lbase + slot * AT_SLOT + ko, vaa = lbase + slot * AT_SLOT + vo;
    const uint32_t kna = lbase + nslot * AT_SLOT + ko, vna = lbase + nslot * AT_SLOT + vo;
    asm volatile(AT_CHUNK_ASM
                 : "+{v[16:31]}"(o0), "+{v[32:47]}"(o1), "+{v[48:63]}"(sc), "+{v[64:79]}"(sb_), "+{v[84:87]}"(kb_),
                   "+{v[88:91]}"(kn), "+{v[92:95]}"(va), "+{v[96:99]}"(vb), "+{v[100:103]}"(vc), "+{v[104:107]}"(vd)
                 : "{v[0:15]}"(negm), "{v[80:83]}"(qf), [ka] "v"(ka), [va] "v"(vaa), [kn] "v"(kna), [vn] "v"(vna) AT_PM
                 : "memory");
    slot = nslot;
  }
  asm volatile(AT_TAIL_ASM
               : "+{v[16:31]}"(o0), "+{v[32:47]}"(o1), "+{v[64:79]}"(sb_), "+{v[84:87]}"(kb_), "+{v[88:91]}"(kn), "+{v[92:95]}"(va),
                 "+{v[96:99]}"(vb), "+{v[100:103]}"(vc), "+{v[104:107]}"(vd)::"memory");
  f32x16 o = o0 + o1;
  {
    bool ok = o[8] > 0x1p-100f && o[8] < 0x1p120f;
#pragma unroll
    for (int r = 0; r < 8; ++r) ok = ok && (fabsf(o[r]) < 0x1p126f);
    if (__builtin_amdgcn_ballot_w64(!ok) != 0) at_true_max(kg, vg, qf, lane, o);
  }
  const float inv = __builtin_amdgcn_rcpf(o[8]);
  const u32x4 ow = {pack2(o[0] * inv, o[1] * inv), pack2(o[2] * inv, o[3] * inv), pack2(o[4] * inv, o[5] * inv), pack2(o[6] * inv, o[7] * inv)};
  st16w(ws + WS_O + ((size_t)tq * 32 + head) * 1024 + lane * 16, __builtin_elementwise_min(ow, u32x4{~1u, ~1u, ~1u, ~1u}));
  f32x16 acc[2];
  f32x4 sb[2];
  f32x16 of0, of1;
  const int xrw = tb * 32 + l31;
  const int xo = AT_X + xrw * 128 + (((4 * fcb + g0) ^ ((xrw >> 1) & 7)) << 4) + 8 * hi;
  u32x2 xr0, xr1;
  {
    const char* ob = ws + WS_O + ((size_t)(tp * 2 + tb) * 32 + kq * 8) * 1024;
    const int g16 = (lane >> 4) & 1, q4 = (lane >> 2) & 3, p4 = lane & 3, row = 4 * hi + q4;
    const int wb = kq < 2 ? AT_WO + kq * 16384 : (kq == 2 ? 2 * AT_SLOT : 0);
    const uint32_t r0 = lbase + wb + row * 128 + (((row >> 1) & 1) << 6) + 32 * g16 + 8 * p4;
    const uint32_t pp = lbase + AT_W + ((tb * 2 + fcb) * 4 + g0) * 1024 + lane * 16;
    const uint32_t xa = lbase + xo;
    uint32_t cnt;
    asm volatile(AT_P5_ASM
                 : "=&{v[96:111]}"(acc[0]), "=&{v[112:127]}"(acc[1]), "=&{v[128:131]}"(sb[0]), "=&{v[132:135]}"(sb[1]), "=&{v[146:147]}"(xr0), "=&{v[148:149]}"(xr1), "=&{v[0:15]}"(of0), "=&{v[16:31]}"(of1), [cnt] "=&s"(cnt) P5_LT
                 : [o0] "v"(lane * 16), [o1] "v"(lane * 16 + 4096), [ob] "s"(ob), [r0] "v"(r0), [r1] "v"(r0 ^ 64), [pp] "v"(pp), [xa] "v"(xa),
                   [dl] "s"(kq == 3 ? AT_WL - 15360 : 0)
                 : "memory", "vcc", "scc", P5_CLOBBER);
  }
#pragma unroll
  for (int cb = 0; cb < 2; ++cb)
#pragma unroll
    for (int g = 0; g < 4; ++g)
      *(__attribute__((address_space(3))) f32x4*)(smem + AT_W + tb * 32768 + (cb * 4 + g) * 4096 + kq * 1024 + lane * 16) =
          f32x4{acc[cb][4 * g], acc[cb][4 * g + 1], acc[cb][4 * g + 2], acc[cb][4 * g + 3]};
  bar();
#pragma unroll
  for (int gg = 0; gg < 2; ++gg) {
    const int oo = AT_W + tb * 32768 + (fcb * 4 + g0 + gg) * 4096 + lane * 16;
    const f32x4 sv = (ldsf4(smem + oo) + ldsf4(smem + oo + 1024)) + (ldsf4(smem + oo + 2048) + ldsf4(smem + oo + 3072));
    const u32x2 xr = gg == 0 ? xr0 : xr1;
    const float xv[4] = {__uint_as_float(xr[0] << 16), __uint_as_float(xr[0] & 0xffff0000u), __uint_as_float(xr[1] << 16),
                         __uint_as_float(xr[1] & 0xffff0000u)};
    const u32x2 pk = {pack2(__builtin_fmaf(sv[0], sb[gg][0], xv[0]), __builtin_fmaf(sv[1], sb[gg][1], xv[1])),
                      pack2(__builtin_fmaf(sv[2], sb[gg][2], xv[2]), __builtin_fmaf(sv[3], sb[gg][3], xv[3]))};
    *(__attribute__((address_space(3))) u32x2*)(smem + (xo ^ (16 * gg))) = pk;
  }
  bar();
  {
    const int row = 8 * w + (lane >> 3);
    char* dst = out + ((size_t)(tp * 64 + row) * DM + cblk * 64) * 2 + (((lane & 7) ^ ((row >> 1) & 7)) << 4);
    ST2(dst, lds128(smem + AT_X + w * 1024 + lane * 16));
  }
}

extern "C" void kernel_launch(void* const* d_in, const int* in_sizes, int n_in, void* d_out, int out_size, void* d_ws,
                              size_t ws_size, hipStream_t stream) {
  const char* x = (const char*)d_in[0];
  const char* wqkv = (const char*)d_in[1];
  const char* wo = (const char*)d_in[2];
  const char* wg = (const char*)d_in[4];
  const char* wu = (const char*)d_in[5];
  const char* wd = (const char*)d_in[6];
  char* ws = (char*)d_ws;
  k_gemm1<<<256, 768, 0, stream>>>(x, wqkv, wg, wu, wd, wo, ws);
  k_attn<<<256, 768, 0, stream>>>(x, wo, wd, ws, (char*)d_out);
}
```
